```python
import torch

def _softmax(x, axis=-1):
    return torch.softmax(x, dim=axis)
def _gelu(x):
    return torch.nn.functional.gelu(x, approximate="tanh")
def _silu(x):
    return torch.nn.functional.silu(x)
def _sigmoid(x):
    return torch.sigmoid(x)
def _layernorm(x, axis=-1, eps=1e-5):
    m = x.mean(dim=axis, keepdim=True)
    v = x.var(dim=axis, keepdim=True, unbiased=False)
    return (x - m) / torch.sqrt(v + eps)
def _scatter_add(tgt, idx, src, axis=0):
    return tgt.clone().index_add_(axis, idx, src)
def _segment_sum(src, seg, n):
    out = torch.zeros((n,) + src.shape[1:], dtype=src.dtype)
    return out.index_add_(0, seg, src)
def _pad(x, pw):
    flat = tuple(v for pair in reversed(pw) for v in pair)
    return torch.nn.functional.pad(x, flat)

kernel_name = 'standalone__paged_block_table_kv_radixattention__sampling'

def setup_inputs():

    _w = (lambda a: (lambda t: t.to(torch.bfloat16) if t.is_floating_point() else t)(torch.as_tensor(a)))
    B, H, Hkv, Dh = 8, 32, 8, 128
    Lk = 4096
    bs = 16
    nb = max(2, Lk // bs)
    P = max(1, nb // 2)
    n_phys = P + B * (nb - P)
    shared = torch.arange(P, dtype=torch.int64)
    priv = P + torch.arange(B * (nb - P), dtype=torch.int64).reshape(B, nb - P)
    bt = torch.cat([torch.tile(shared[None, :], (B, 1)), priv], dim=1)
    ctx = nb * bs - (torch.arange(1, B + 1, dtype=torch.int64) * 2654435761) % max(1, (nb * bs - P * bs) // 2)
    return {
        "q": _w((lambda *s: torch.randn(*s, dtype=torch.bfloat16) * 0.8)(B, H, Dh)),
        "k_cache": _w((lambda *s: torch.randn(*s, dtype=torch.bfloat16) * 0.8)(n_phys, bs, Hkv, Dh)),
        "v_cache": _w((lambda *s: torch.randn(*s, dtype=torch.bfloat16) * 0.05)(n_phys, bs, Hkv, Dh)),
        "block_table": _w(bt.to(torch.int64)),
        "ctx_lens": _w(torch.as_tensor(ctx).to(torch.int64)),
    }

def reference(q, k_cache, v_cache, block_table, ctx_lens):
    B, H, Dh = q.shape
    n_phys, bs, Hkv, _ = k_cache.shape
    nb = block_table.shape[1]
    Lk = nb * bs
    rep = H // Hkv
    k = k_cache[block_table].reshape(B, Lk, Hkv, Dh)
    v = v_cache[block_table].reshape(B, Lk, Hkv, Dh)
    k = (lambda x, n, axis: torch.repeat_interleave(x, n, dim=axis))(k, rep, axis=2)
    v = (lambda x, n, axis: torch.repeat_interleave(x, n, dim=axis))(v, rep, axis=2)
    scores = torch.einsum("bhd,bkhd->bhk", q, k) * (Dh ** -0.5)
    pos = torch.arange(Lk)
    valid = pos[None, :] < ctx_lens[:, None]
    scores = torch.where(valid[:, None, :], scores, -1e9)
    p = _softmax(scores, axis=-1)
    return torch.einsum("bhk,bkhd->bhd", p, v)
```

```cpp
#include <hip/hip_runtime.h>
#include <hip/hip_bf16.h>
#include <stdint.h>

typedef __attribute__((ext_vector_type(8))) __bf16 bf16x8;
typedef __attribute__((ext_vector_type(4))) float f32x4;
typedef __attribute__((ext_vector_type(4))) unsigned u32x4;

namespace {
constexpr int NB = 8, NH = 32, NKV = 8, HD = 128, NPG = 256, PS = 16, LK = NPG * PS;
constexpr int WGH = 32;
constexpr int NW = 8;
constexpr int NP = 4;
constexpr int OST = 132;
constexpr float SCALE = 0.08838834764831845f;
constexpr float C2 = SCALE * 1.4426950408889634f;
constexpr float MASKV = -998244352.0f;
#define KV_AUX 2

constexpr int L_BT = 0;
constexpr int L_FIRST = L_BT + NB * NPG;
constexpr int L_M = L_FIRST + 8;
constexpr int L_L = L_M + 2 * NW * 32;
constexpr int L_O = L_L + NW * 32;
constexpr int L_END = L_O + NW * 32 * OST;

constexpr size_t W_ML = 0;
constexpr size_t W_O = W_ML + (size_t)NKV * WGH * 32 * 2;

__device__ __forceinline__ float xg_max(float x) {
  auto s = __builtin_amdgcn_permlane16_swap(__float_as_uint(x), __float_as_uint(x), false, false);
  x = fmaxf(__uint_as_float(s[0]), __uint_as_float(s[1]));
  auto t = __builtin_amdgcn_permlane32_swap(__float_as_uint(x), __float_as_uint(x), false, false);
  return fmaxf(__uint_as_float(t[0]), __uint_as_float(t[1]));
}
__device__ __forceinline__ float xg_sum(float x) {
  auto s = __builtin_amdgcn_permlane16_swap(__float_as_uint(x), __float_as_uint(x), false, false);
  x = __uint_as_float(s[0]) + __uint_as_float(s[1]);
  auto t = __builtin_amdgcn_permlane32_swap(__float_as_uint(x), __float_as_uint(x), false, false);
  return __uint_as_float(t[0]) + __uint_as_float(t[1]);
}
__device__ __forceinline__ unsigned pk_bf16(float lo, float hi) {
  typedef __attribute__((ext_vector_type(2))) __bf16 bf16x2;
  bf16x2 v = {(__bf16)lo, (__bf16)hi};
  return __builtin_bit_cast(unsigned, v);
}
__device__ __forceinline__ f32x4 mfma(u32x4 a, u32x4 b, f32x4 c) {
  return __builtin_amdgcn_mfma_f32_16x16x32_bf16(__builtin_bit_cast(bf16x8, a), __builtin_bit_cast(bf16x8, b), c, 0, 0, 0);
}

__global__ __launch_bounds__(NW * 64, 2) void attn_split(const unsigned short* __restrict__ q, const unsigned short* __restrict__ kc,
                                                         const unsigned short* __restrict__ vc, const int* __restrict__ bt,
                                                         const int* __restrict__ lens, float* __restrict__ ws, unsigned kv_bytes) {
  __shared__ __attribute__((aligned(16))) float lds[L_END];
  int* ldsi = reinterpret_cast<int*>(lds);
  const int tid = threadIdx.x, lane = tid & 63, wave = __builtin_amdgcn_readfirstlane(tid >> 6);
  const int g = lane >> 4, c = lane & 15;
  const int kvh = blockIdx.x & (NKV - 1), wi = blockIdx.x >> 3;

  const int col = tid & (NPG - 1), rb = (tid >> 8) * 4;
  const int t0 = bt[col];
  int tv[4];
#pragma unroll
  for (int i = 0; i < 4; ++i) tv[i] = bt[(rb + i) * NPG + col];
  u32x4 qf[2][4];
#pragma unroll
  for (int t = 0; t < 2; ++t) {
    const int row = 16 * t + c;
    const unsigned short* qp = q + ((row >> 2) * NH + kvh * 4 + (row & 3)) * HD + 8 * g;
#pragma unroll
    for (int s = 0; s < 4; ++s) qf[t][s] = *reinterpret_cast<const u32x4*>(qp + 32 * s);
  }
  int len[NB];
#pragma unroll
  for (int b = 0; b < NB; ++b) len[b] = min(max(lens[b], 0), LK);
  {
    const bool ne = (tv[0] != t0) | (tv[1] != t0) | (tv[2] != t0) | (tv[3] != t0);
    const unsigned long long m = __ballot(ne);
#pragma unroll
    for (int i = 0; i < 4; ++i) ldsi[L_BT + (rb + i) * NPG + col] = tv[i];
    if (lane == 0) ldsi[L_FIRST + wave] = m ? (wave & 3) * 64 + __builtin_ctzll(m) : NPG;
  }
  __syncthreads();
  int S = NPG;
#pragma unroll
  for (int w = 0; w < NW; ++w) S = min(S, ldsi[L_FIRST + w]);
#pragma unroll
  for (int b = 0; b < NB; ++b) S = min(S, len[b] >> 4);
  S = __builtin_amdgcn_readfirstlane(S);
  int off[NB + 1];
  off[0] = S;
#pragma unroll
  for (int b = 0; b < NB; ++b) off[b + 1] = off[b] + ((len[b] + 15) >> 4) - S;
  const int U = off[NB];
  const int u0 = (wi * U) / WGH, u1 = ((wi + 1) * U) / WGH;

  const __amdgpu_buffer_rsrc_t krs = __builtin_amdgcn_make_buffer_rsrc((void*)(kc + kvh * HD), 0, kv_bytes - kvh * HD * 2, 0x00020000);
  const __amdgpu_buffer_rsrc_t vrs = __builtin_amdgcn_make_buffer_rsrc((void*)(vc + kvh * HD), 0, kv_bytes - kvh * HD * 2, 0x00020000);
  const int koff = (4 * (c & 3) + (c >> 2)) * (NKV * HD * 2) + g * 16;
  const int voff = g * (NKV * HD * 2) + c * 16;

  int rlo = 0, rhi = 32;
  if (u0 >= S) {
    int blo = 0, bhi = 0;
#pragma unroll
    for (int b = 1; b < NB; ++b) {
      if (u0 >= off[b]) blo = b;
      if (u1 - 1 >= off[b]) bhi = b;
    }
    rlo = 4 * blo;
    rhi = 4 * bhi + 4;
  }
  if (u1 <= u0) rhi = rlo = 0;
  float M[2] = {-INFINITY, -INFINITY}, l[2] = {0.f, 0.f};

  unsigned so_v;
  int info_v;
  {
    const int u = u0 + wave + NW * lane;
    int bsel = 0, base = off[0], lb = len[0];
#pragma unroll
    for (int b = 1; b < NB; ++b)
      if (u >= off[b]) { bsel = b; base = off[b]; lb = len[b]; }
    const bool sh = u < S;
    const int j = min(sh ? u : S + u - base, NPG - 1);
    const int phys = ldsi[L_BT + (sh ? 0 : bsel) * NPG + j];
    so_v = (u < u1) ? (unsigned)phys * (PS * NKV * HD * 2) : 0x80000000u;
    info_v = (u < u1) ? ((sh ? 16 : min(16, lb - 16 * j)) | ((sh ? 8 : bsel) << 8)) : 0;
  }
  const int nrd = (u1 - u0 + NW * NP - 1) / (NW * NP);
  for (int rd = 0; rd < nrd; ++rd) {
    int par = rd;
    asm volatile("" : "+s"(par));
    unsigned so[NP];
    int info[NP];
#pragma unroll
    for (int p = 0; p < NP; ++p) {
      so[p] = __builtin_amdgcn_readlane(so_v, NP * par + p);
      info[p] = __builtin_amdgcn_readlane(info_v, NP * par + p);
    }
    u32x4 kf[NP][4], vf[NP][4];
#pragma unroll
    for (int p = 0; p < NP; ++p)
#pragma unroll
      for (int s = 0; s < 4; ++s) kf[p][s] = __builtin_amdgcn_raw_buffer_load_b128(krs, koff + 64 * s, so[p], KV_AUX);
#pragma unroll
    for (int p = 0; p < NP; ++p)
#pragma unroll
      for (int i = 0; i < 4; ++i) vf[p][i] = __builtin_amdgcn_raw_buffer_load_b128(vrs, voff, so[p] + i * (4 * NKV * HD * 2), KV_AUX);
    __builtin_amdgcn_sched_barrier(0);

    f32x4 sc[NP][2];
    float mx[2] = {-INFINITY, -INFINITY};
#pragma unroll
    for (int p = 0; p < NP; ++p) {
      const int nv = info[p] & 255, sq = info[p] >> 8;
#pragma unroll
      for (int t = 0; t < 2; ++t) {
        f32x4 a = {0.f, 0.f, 0.f, 0.f};
#pragma unroll
        for (int s = 0; s < 4; ++s) a = mfma(kf[p][s], qf[t][s], a);
        const bool cv = (sq == 8) | (sq == 4 * t + (c >> 2));
#pragma unroll
        for (int r = 0; r < 4; ++r) {
          a[r] = (cv & (4 * r + g < nv)) ? a[r] : -INFINITY;
          mx[t] = fmaxf(mx[t], a[r]);
        }
        sc[p][t] = a;
      }
    }
#pragma unroll
    for (int t = 0; t < 2; ++t) mx[t] = xg_max(mx[t]);
    if (g < 2) lds[L_M + (par * NW + wave) * 32 + 16 * g + c] = g ? mx[1] : mx[0];
    __syncthreads();
    u32x4 pf[NP / 2][2];
    float al[2];
#pragma unroll
    for (int t = 0; t < 2; ++t) {
      float mn = M[t];
#pragma unroll
      for (int w = 0; w < NW; ++w) mn = fmaxf(mn, lds[L_M + (par * NW + w) * 32 + 16 * t + c]);
      const float ms = (mn == -INFINITY) ? 0.f : mn * C2;
      const float alpha = __builtin_amdgcn_exp2f(M[t] * C2 - ms);
      M[t] = mn;
      float ls = l[t] * alpha;
      al[t] = alpha;
#pragma unroll
      for (int p = 0; p < NP; ++p) {
        float e[4];
#pragma unroll
        for (int r = 0; r < 4; ++r) { e[r] = __builtin_amdgcn_exp2f(fmaf(sc[p][t][r], C2, -ms)); ls += e[r]; }
        pf[p >> 1][t][(p & 1) * 2] = pk_bf16(e[0], e[1]);
        pf[p >> 1][t][(p & 1) * 2 + 1] = pk_bf16(e[2], e[3]);
      }
      l[t] = ls;
    }
    f32x4 acc[2][8];
#pragma unroll
    for (int t = 0; t < 2; ++t)
#pragma unroll
      for (int d = 0; d < 8; ++d) acc[t][d] = f32x4{0.f, 0.f, 0.f, 0.f};
#pragma unroll
    for (int pp = 0; pp < NP / 2; ++pp) {
#pragma unroll
      for (int e = 0; e < 4; ++e) {
        u32x4 lo, hi;
#pragma unroll
        for (int h = 0; h < 2; ++h) {
          const u32x4* v = vf[2 * pp + h];
          lo[2 * h] = __builtin_amdgcn_perm(v[1][e], v[0][e], 0x05040100u);
          lo[2 * h + 1] = __builtin_amdgcn_perm(v[3][e], v[2][e], 0x05040100u);
          hi[2 * h] = __builtin_amdgcn_perm(v[1][e], v[0][e], 0x07060302u);
          hi[2 * h + 1] = __builtin_amdgcn_perm(v[3][e], v[2][e], 0x07060302u);
        }
#pragma unroll
        for (int t = 0; t < 2; ++t) {
          acc[t][2 * e] = mfma(lo, pf[pp][t], acc[t][2 * e]);
          acc[t][2 * e + 1] = mfma(hi, pf[pp][t], acc[t][2 * e + 1]);
        }
      }
    }
#pragma unroll
    for (int t = 0; t < 2; ++t) {
      const int row = 16 * t + c;
      if (row >= rlo && row < rhi) {
        f32x4* op = reinterpret_cast<f32x4*>(&lds[L_O + (wave * 32 + row) * OST + 32 * g]);
        if (par != 0) {
#pragma unroll
          for (int d = 0; d < 8; ++d) acc[t][d] += op[d] * al[t];
        }
#pragma unroll
        for (int d = 0; d < 8; ++d) op[d] = acc[t][d];
      }
    }
  }

#pragma unroll
  for (int t = 0; t < 2; ++t) {
    const float ls = xg_sum(l[t]);
    const int row = 16 * t + c;
    if (g == 0) lds[L_L + wave * 32 + row] = ls;
  }
  __syncthreads();
  const size_t slot = (size_t)(kvh * WGH + wi) * 32;
  for (int it = tid; it < (rhi - rlo) * 32; it += NW * 64) {
    const int row = rlo + (it >> 5), x = it & 31;
    f32x4 o = {0.f, 0.f, 0.f, 0.f};
#pragma unroll
    for (int w = 0; w < NW; ++w) o += *reinterpret_cast<const f32x4*>(&lds[L_O + (w * 32 + row) * OST + 4 * x]);
    *reinterpret_cast<f32x4*>(ws + W_O + (slot + row) * HD + 4 * x) = o;
  }
  if (tid < 32) {
    float L = 0.f;
#pragma unroll
    for (int w = 0; w < NW; ++w) L += lds[L_L + w * 32 + tid];
    const bool ok = tid >= rlo && tid < rhi;
    *reinterpret_cast<float2*>(ws + W_ML + (slot + tid) * 2) = make_float2(g ? M[1] : M[0], ok ? L : 0.f);
  }
}

__global__ __launch_bounds__(64) void attn_combine(const unsigned short* __restrict__ vc, const int* __restrict__ bt, const int* __restrict__ lens,
                                                   const float* __restrict__ ws, unsigned short* __restrict__ out) {
  const int lane = threadIdx.x, kvh = blockIdx.x & (NKV - 1), row = blockIdx.x >> 3, b = row >> 2;
  const int hw = lane >> 5, x = lane & 31;
  const size_t slot0 = (size_t)kvh * WGH * 32 + row;
  const float2 ml = *reinterpret_cast<const float2*>(ws + W_ML + (slot0 + (size_t)x * 32) * 2);
  const bool ok = ml.y > 0.f;
  float M = ok ? ml.x : -INFINITY;
#pragma unroll
  for (int s = 1; s < 32; s <<= 1) M = fmaxf(M, __shfl_xor(M, s));
  const float wgt = ok ? __builtin_amdgcn_exp2f((ml.x - M) * C2) : 0.f;
  float L = wgt * ml.y;
#pragma unroll
  for (int s = 1; s < 32; s <<= 1) L += __shfl_xor(L, s);
  const __amdgpu_buffer_rsrc_t ors = __builtin_amdgcn_make_buffer_rsrc((void*)(ws + W_O + slot0 * HD), 0, (unsigned)(((WGH - 1) * 32 + 1) * HD * 4), 0x00020000);
  const unsigned long long okm = __ballot(ok);
  u32x4 ov[WGH / 2];
#pragma unroll
  for (int k = 0; k < WGH / 2; ++k) {
    const int i = 2 * k + hw;
    const unsigned vo = ((okm >> i) & 1) ? (unsigned)(i * 32 * HD * 4 + x * 16) : 0x80000000u;
    ov[k] = __builtin_amdgcn_raw_buffer_load_b128(ors, vo, 0, 0);
  }
  f32x4 o = {0.f, 0.f, 0.f, 0.f};
#pragma unroll
  for (int k = 0; k < WGH / 2; ++k) {
    const float w = __shfl(wgt, 2 * k + hw);
    o += w * __builtin_bit_cast(f32x4, ov[k]);
  }
#pragma unroll
  for (int r = 0; r < 4; ++r) o[r] += __shfl_xor(o[r], 32);
  const int d0 = 32 * (x >> 3) + (x & 7);
  const int len = min(max(lens[b], 0), LK);
  if (len < LK && !(M * SCALE >= MASKV + 256.f)) {
    const float Mn = M * SCALE, Mx = fmaxf(Mn, MASKV);
    const float wv = (L > 0.f) ? __expf(Mn - Mx) : 0.f, wm = __expf(MASKV - Mx);
    f32x4 sv = {0.f, 0.f, 0.f, 0.f};
    for (int pos = len; pos < LK; ++pos) {
      const unsigned short* vp = vc + ((size_t)bt[b * NPG + (pos >> 4)] * PS + (pos & 15)) * (NKV * HD) + kvh * HD + d0;
#pragma unroll
      for (int r = 0; r < 4; ++r) sv[r] += __uint_as_float((unsigned)vp[8 * r] << 16);
    }
    o = o * wv + sv * wm;
    L = L * wv + wm * (float)(LK - len);
  }
  const float inv = 1.f / L;
  if (hw == 0) {
    unsigned short* op = out + (b * NH + kvh * 4 + (row & 3)) * HD + d0;
#pragma unroll
    for (int r = 0; r < 4; ++r) op[8 * r] = __builtin_bit_cast(unsigned short, (__bf16)(o[r] * inv));
  }
}
}

extern "C" void kernel_launch(void* const* d_in, const int* in_sizes, int n_in, void* d_out, int out_size, void* d_ws, size_t ws_size,
                              hipStream_t stream) {
  const unsigned short* q = (const unsigned short*)d_in[0];
  const unsigned short* kc = (const unsigned short*)d_in[1];
  const unsigned short* vc = (const unsigned short*)d_in[2];
  const int* bt = (const int*)d_in[3];
  const int* lens = (const int*)d_in[4];
  const unsigned kv_bytes = (unsigned)in_sizes[1] * 2u;
  attn_split<<<NKV * WGH, NW * 64, 0, stream>>>(q, kc, vc, bt, lens, (float*)d_ws, kv_bytes);
  attn_combine<<<NKV * 32, 64, 0, stream>>>(vc, bt, lens, (const float*)d_ws, (unsigned short*)d_out);
}
```

```cpp
#include <hip/hip_runtime.h>
#include <hip/hip_bf16.h>
#include <stdint.h>

typedef __attribute__((ext_vector_type(8))) __bf16 bf16x8;
typedef __attribute__((ext_vector_type(4))) float f32x4;
typedef __attribute__((ext_vector_type(4))) unsigned u32x4;

#define STAMP(i)
#define STAMP_DECL
#define STAMP_FLUSH(ws, slot)

namespace {
constexpr int NB = 8, NH = 32, NKV = 8, HD = 128, NPG = 256, PS = 16, LK = NPG * PS;
constexpr int WGH = 32;
constexpr int NW = 8;
constexpr int NP = 4;
constexpr int OST = 132;
constexpr float SCALE = 0.08838834764831845f;
constexpr float C2 = SCALE * 1.4426950408889634f;
constexpr float MASKV = -998244352.0f;
#define KV_AUX 2

constexpr int L_BT = 0;
constexpr int L_FIRST = L_BT + NB * NPG;
constexpr int L_M = L_FIRST + 8;
constexpr int L_L = L_M + 2 * NW * 32;
constexpr int L_O = L_L + NW * 32;
constexpr int L_END = L_O + NW * 32 * OST;

constexpr size_t W_ML = 0;
constexpr size_t W_O = W_ML + (size_t)NKV * WGH * 32 * 2;

__device__ __forceinline__ float xg_max(float x) {
  auto s = __builtin_amdgcn_permlane16_swap(__float_as_uint(x), __float_as_uint(x), false, false);
  x = fmaxf(__uint_as_float(s[0]), __uint_as_float(s[1]));
  auto t = __builtin_amdgcn_permlane32_swap(__float_as_uint(x), __float_as_uint(x), false, false);
  return fmaxf(__uint_as_float(t[0]), __uint_as_float(t[1]));
}
__device__ __forceinline__ float xg_sum(float x) {
  auto s = __builtin_amdgcn_permlane16_swap(__float_as_uint(x), __float_as_uint(x), false, false);
  x = __uint_as_float(s[0]) + __uint_as_float(s[1]);
  auto t = __builtin_amdgcn_permlane32_swap(__float_as_uint(x), __float_as_uint(x), false, false);
  return __uint_as_float(t[0]) + __uint_as_float(t[1]);
}
__device__ __forceinline__ unsigned pk_bf16(float lo, float hi) {
  typedef __attribute__((ext_vector_type(2))) __bf16 bf16x2;
  bf16x2 v = {(__bf16)lo, (__bf16)hi};
  return __builtin_bit_cast(unsigned, v);
}
__device__ __forceinline__ f32x4 mfma(u32x4 a, u32x4 b, f32x4 c) {
  return __builtin_amdgcn_mfma_f32_16x16x32_bf16(__builtin_bit_cast(bf16x8, a), __builtin_bit_cast(bf16x8, b), c, 0, 0, 0);
}

__global__ __launch_bounds__(NW * 64, 2) void attn_split(const unsigned short* __restrict__ q, const unsigned short* __restrict__ kc,
                                                         const unsigned short* __restrict__ vc, const int* __restrict__ bt,
                                                         const int* __restrict__ lens, float* __restrict__ ws, unsigned kv_bytes) {
  __shared__ __attribute__((aligned(16))) float lds[L_END];
  int* ldsi = reinterpret_cast<int*>(lds);
  STAMP_DECL;
  STAMP(0);
  const int tid = threadIdx.x, lane = tid & 63, wave = __builtin_amdgcn_readfirstlane(tid >> 6);
  const int g = lane >> 4, c = lane & 15;
  const int kvh = blockIdx.x >> 5, wi = blockIdx.x & 31;

  const int col = tid & (NPG - 1), rb = (tid >> 8) * 4;
  const int t0 = bt[col];
  int tv[4];
#pragma unroll
  for (int i = 0; i < 4; ++i) tv[i] = bt[(rb + i) * NPG + col];
  u32x4 qf[2][4];
#pragma unroll
  for (int t = 0; t < 2; ++t) {
    const int row = 16 * t + c;
    const unsigned short* qp = q + ((row >> 2) * NH + kvh * 4 + (row & 3)) * HD + 8 * g;
#pragma unroll
    for (int s = 0; s < 4; ++s) qf[t][s] = *reinterpret_cast<const u32x4*>(qp + 32 * s);
  }
  int len[NB];
#pragma unroll
  for (int b = 0; b < NB; ++b) len[b] = min(max(lens[b], 0), LK);
  {
    const bool ne = (tv[0] != t0) | (tv[1] != t0) | (tv[2] != t0) | (tv[3] != t0);
    const unsigned long long m = __ballot(ne);
#pragma unroll
    for (int i = 0; i < 4; ++i) ldsi[L_BT + (rb + i) * NPG + col] = tv[i];
    if (lane == 0) ldsi[L_FIRST + wave] = m ? (wave & 3) * 64 + __builtin_ctzll(m) : NPG;
  }
  STAMP(1);
  __syncthreads();
  STAMP(2);
  int S = NPG;
#pragma unroll
  for (int w = 0; w < NW; ++w) S = min(S, ldsi[L_FIRST + w]);
#pragma unroll
  for (int b = 0; b < NB; ++b) S = min(S, len[b] >> 4);
  S = __builtin_amdgcn_readfirstlane(S);
  int off[NB + 1];
  off[0] = S;
#pragma unroll
  for (int b = 0; b < NB; ++b) off[b + 1] = off[b] + ((len[b] + 15) >> 4) - S;
  const int U = off[NB];
  const int u0 = (wi * U) / WGH, u1 = ((wi + 1) * U) / WGH;

  const __amdgpu_buffer_rsrc_t krs = __builtin_amdgcn_make_buffer_rsrc((void*)(kc + kvh * HD), 0, kv_bytes - kvh * HD * 2, 0x00020000);
  const __amdgpu_buffer_rsrc_t vrs = __builtin_amdgcn_make_buffer_rsrc((void*)(vc + kvh * HD), 0, kv_bytes - kvh * HD * 2, 0x00020000);
  const int koff = (4 * (c & 3) + (c >> 2)) * (NKV * HD * 2) + g * 16;
  const int voff = g * (NKV * HD * 2) + c * 16;

  int rlo = 0, rhi = 32;
  if (u0 >= S) {
    int blo = 0, bhi = 0;
#pragma unroll
    for (int b = 1; b < NB; ++b) {
      if (u0 >= off[b]) blo = b;
      if (u1 - 1 >= off[b]) bhi = b;
    }
    rlo = 4 * blo;
    rhi = 4 * bhi + 4;
  }
  if (u1 <= u0) rhi = rlo = 0;
  float M[2] = {-INFINITY, -INFINITY}, l[2] = {0.f, 0.f};

  unsigned so_v;
  int info_v;
  {
    const int u = u0 + wave + NW * lane;
    int bsel = 0, base = off[0], lb = len[0];
#pragma unroll
    for (int b = 1; b < NB; ++b)
      if (u >= off[b]) { bsel = b; base = off[b]; lb = len[b]; }
    const bool sh = u < S;
    const int j = min(sh ? u : S + u - base, NPG - 1);
    const int phys = ldsi[L_BT + (sh ? 0 : bsel) * NPG + j];
    so_v = (u < u1) ? (unsigned)phys * (PS * NKV * HD * 2) : 0x80000000u;
    info_v = (u < u1) ? ((sh ? 16 : min(16, lb - 16 * j)) | ((sh ? 8 : bsel) << 8)) : 0;
  }
  const int nrd = (u1 - u0 + NW * NP - 1) / (NW * NP);
  for (int rd = 0; rd < nrd; ++rd) {
    int par = rd;
    asm volatile("" : "+s"(par));
    unsigned so[NP];
    int info[NP];
#pragma unroll
    for (int p = 0; p < NP; ++p) {
      so[p] = __builtin_amdgcn_readlane(so_v, NP * par + p);
      info[p] = __builtin_amdgcn_readlane(info_v, NP * par + p);
    }
    u32x4 kf[NP][4], vf[NP][4];
    STAMP(3);
#pragma unroll
    for (int p = 0; p < NP; ++p)
#pragma unroll
      for (int s = 0; s < 4; ++s) kf[p][s] = __builtin_amdgcn_raw_buffer_load_b128(krs, koff + 64 * s, so[p], KV_AUX);
#pragma unroll
    for (int p = 0; p < NP; ++p)
#pragma unroll
      for (int i = 0; i < 4; ++i) vf[p][i] = __builtin_amdgcn_raw_buffer_load_b128(vrs, voff, so[p] + i * (4 * NKV * HD * 2), KV_AUX);
    __builtin_amdgcn_sched_barrier(0);
    STAMP(4);

    f32x4 sc[NP][2];
    float mx[2] = {-INFINITY, -INFINITY};
#pragma unroll
    for (int p = 0; p < NP; ++p) {
      const int nv = info[p] & 255, sq = info[p] >> 8;
#pragma unroll
      for (int t = 0; t < 2; ++t) {
        f32x4 a = {0.f, 0.f, 0.f, 0.f};
#pragma unroll
        for (int s = 0; s < 4; ++s) a = mfma(kf[p][s], qf[t][s], a);
        const bool cv = (sq == 8) | (sq == 4 * t + (c >> 2));
#pragma unroll
        for (int r = 0; r < 4; ++r) {
          a[r] = (cv & (4 * r + g < nv)) ? a[r] : -INFINITY;
          mx[t] = fmaxf(mx[t], a[r]);
        }
        sc[p][t] = a;
      }
    }
#pragma unroll
    for (int t = 0; t < 2; ++t) mx[t] = xg_max(mx[t]);
    if (g < 2) lds[L_M + (par * NW + wave) * 32 + 16 * g + c] = g ? mx[1] : mx[0];
    STAMP(5);
    __syncthreads();
    STAMP(6);
    u32x4 pf[NP / 2][2];
    float al[2];
#pragma unroll
    for (int t = 0; t < 2; ++t) {
      float mn = M[t];
#pragma unroll
      for (int w = 0; w < NW; ++w) mn = fmaxf(mn, lds[L_M + (par * NW + w) * 32 + 16 * t + c]);
      const float ms = (mn == -INFINITY) ? 0.f : mn * C2;
      const float alpha = __builtin_amdgcn_exp2f(M[t] * C2 - ms);
      M[t] = mn;
      float ls = l[t] * alpha;
      al[t] = alpha;
#pragma unroll
      for (int p = 0; p < NP; ++p) {
        float e[4];
#pragma unroll
        for (int r = 0; r < 4; ++r) { e[r] = __builtin_amdgcn_exp2f(fmaf(sc[p][t][r], C2, -ms)); ls += e[r]; }
        pf[p >> 1][t][(p & 1) * 2] = pk_bf16(e[0], e[1]);
        pf[p >> 1][t][(p & 1) * 2 + 1] = pk_bf16(e[2], e[3]);
      }
      l[t] = ls;
    }
    f32x4 acc[2][8];
#pragma unroll
    for (int t = 0; t < 2; ++t)
#pragma unroll
      for (int d = 0; d < 8; ++d) acc[t][d] = f32x4{0.f, 0.f, 0.f, 0.f};
#pragma unroll
    for (int pp = 0; pp < NP / 2; ++pp) {
#pragma unroll
      for (int e = 0; e < 4; ++e) {
        u32x4 lo, hi;
#pragma unroll
        for (int h = 0; h < 2; ++h) {
          const u32x4* v = vf[2 * pp + h];
          lo[2 * h] = __builtin_amdgcn_perm(v[1][e], v[0][e], 0x05040100u);
          lo[2 * h + 1] = __builtin_amdgcn_perm(v[3][e], v[2][e], 0x05040100u);
          hi[2 * h] = __builtin_amdgcn_perm(v[1][e], v[0][e], 0x07060302u);
          hi[2 * h + 1] = __builtin_amdgcn_perm(v[3][e], v[2][e], 0x07060302u);
        }
#pragma unroll
        for (int t = 0; t < 2; ++t) {
          acc[t][2 * e] = mfma(lo, pf[pp][t], acc[t][2 * e]);
          acc[t][2 * e + 1] = mfma(hi, pf[pp][t], acc[t][2 * e + 1]);
        }
      }
    }
    STAMP(7);
#pragma unroll
    for (int t = 0; t < 2; ++t) {
      const int row = 16 * t + c;
      if (row >= rlo && row < rhi) {
        f32x4* op = reinterpret_cast<f32x4*>(&lds[L_O + (wave * 32 + row) * OST + 32 * g]);
        if (par != 0) {
#pragma unroll
          for (int d = 0; d < 8; ++d) acc[t][d] += op[d] * al[t];
        }
#pragma unroll
        for (int d = 0; d < 8; ++d) op[d] = acc[t][d];
      }
    }
  }

#pragma unroll
  for (int t = 0; t < 2; ++t) {
    const float ls = xg_sum(l[t]);
    const int row = 16 * t + c;
    if (g == 0) lds[L_L + wave * 32 + row] = ls;
  }
  STAMP(8);
  __syncthreads();
  STAMP(9);
  const size_t slot = (size_t)(kvh * WGH + wi) * 32;
  for (int it = tid; it < (rhi - rlo) * 32; it += NW * 64) {
    const int row = rlo + (it >> 5), x = it & 31;
    f32x4 o = {0.f, 0.f, 0.f, 0.f};
#pragma unroll
    for (int w = 0; w < NW; ++w) o += *reinterpret_cast<const f32x4*>(&lds[L_O + (w * 32 + row) * OST + 4 * x]);
    *reinterpret_cast<f32x4*>(ws + W_O + (slot + row) * HD + 4 * x) = o;
  }
  if (tid < 32) {
    float L = 0.f;
#pragma unroll
    for (int w = 0; w < NW; ++w) L += lds[L_L + w * 32 + tid];
    const bool ok = tid >= rlo && tid < rhi;
    *reinterpret_cast<float2*>(ws + W_ML + (slot + tid) * 2) = make_float2(g ? M[1] : M[0], ok ? L : 0.f);
  }
  STAMP_FLUSH(ws, blockIdx.x * NW + wave);
}

__global__ __launch_bounds__(64) void attn_combine(const unsigned short* __restrict__ vc, const int* __restrict__ bt, const int* __restrict__ lens,
                                                   const float* __restrict__ ws, unsigned short* __restrict__ out) {
  const int lane = threadIdx.x, kvh = blockIdx.x & (NKV - 1), row = blockIdx.x >> 3, b = row >> 2;
  const int hw = lane >> 5, x = lane & 31;
  STAMP_DECL;
  STAMP(0);
  const size_t slot0 = (size_t)kvh * WGH * 32 + row;
  const float2 ml = *reinterpret_cast<const float2*>(ws + W_ML + (slot0 + (size_t)x * 32) * 2);
  const bool ok = ml.y > 0.f;
  STAMP(1);
  float M = ok ? ml.x : -INFINITY;
#pragma unroll
  for (int s = 1; s < 32; s <<= 1) M = fmaxf(M, __shfl_xor(M, s));
  const float wgt = ok ? __builtin_amdgcn_exp2f((ml.x - M) * C2) : 0.f;
  float L = wgt * ml.y;
#pragma unroll
  for (int s = 1; s < 32; s <<= 1) L += __shfl_xor(L, s);
  const __amdgpu_buffer_rsrc_t ors = __builtin_amdgcn_make_buffer_rsrc((void*)(ws + W_O + slot0 * HD), 0, (unsigned)(((WGH - 1) * 32 + 1) * HD * 4), 0x00020000);
  const unsigned long long okm = __ballot(ok);
  u32x4 ov[WGH / 2];
#pragma unroll
  for (int k = 0; k < WGH / 2; ++k) {
    const int i = 2 * k + hw;
    const unsigned vo = ((okm >> i) & 1) ? (unsigned)(i * 32 * HD * 4 + x * 16) : 0x80000000u;
    ov[k] = __builtin_amdgcn_raw_buffer_load_b128(ors, vo, 0, 0);
  }
  f32x4 o = {0.f, 0.f, 0.f, 0.f};
#pragma unroll
  for (int k = 0; k < WGH / 2; ++k) {
    const float w = __shfl(wgt, 2 * k + hw);
    o += w * __builtin_bit_cast(f32x4, ov[k]);
  }
#pragma unroll
  for (int r = 0; r < 4; ++r) o[r] += __shfl_xor(o[r], 32);
  STAMP(2);
  const int d0 = 32 * (x >> 3) + (x & 7);
  const int len = min(max(lens[b], 0), LK);
  if (len < LK && !(M * SCALE >= MASKV + 256.f)) {
    const float Mn = M * SCALE, Mx = fmaxf(Mn, MASKV);
    const float wv = (L > 0.f) ? __expf(Mn - Mx) : 0.f, wm = __expf(MASKV - Mx);
    f32x4 sv = {0.f, 0.f, 0.f, 0.f};
    for (int pos = len; pos < LK; ++pos) {
      const unsigned short* vp = vc + ((size_t)bt[b * NPG + (pos >> 4)] * PS + (pos & 15)) * (NKV * HD) + kvh * HD + d0;
#pragma unroll
      for (int r = 0; r < 4; ++r) sv[r] += __uint_as_float((unsigned)vp[8 * r] << 16);
    }
    o = o * wv + sv * wm;
    L = L * wv + wm * (float)(LK - len);
  }
  const float inv = 1.f / L;
  if (hw == 0) {
    unsigned short* op = out + (b * NH + kvh * 4 + (row & 3)) * HD + d0;
#pragma unroll
    for (int r = 0; r < 4; ++r) op[8 * r] = __builtin_bit_cast(unsigned short, (__bf16)(o[r] * inv));
  }
  STAMP_FLUSH(ws, 2048 + blockIdx.x);
}
}

extern "C" void kernel_launch(void* const* d_in, const int* in_sizes, int n_in, void* d_out, int out_size, void* d_ws, size_t ws_size,
                              hipStream_t stream) {
  const unsigned short* q = (const unsigned short*)d_in[0];
  const unsigned short* kc = (const unsigned short*)d_in[1];
  const unsigned short* vc = (const unsigned short*)d_in[2];
  const int* bt = (const int*)d_in[3];
  const int* lens = (const int*)d_in[4];
  const unsigned kv_bytes = (unsigned)in_sizes[1] * 2u;
  attn_split<<<NKV * WGH, NW * 64, 0, stream>>>(q, kc, vc, bt, lens, (float*)d_ws, kv_bytes);
  attn_combine<<<NKV * 32, 64, 0, stream>>>(vc, bt, lens, (const float*)d_ws, (unsigned short*)d_out);
}
```

```cpp
#define STAMPS
#define DIAG_K 0
#define DIAG_I 0
#define DIAG_MIN 1
#define DIAG_N1 256
#include <hip/hip_runtime.h>
#include <hip/hip_bf16.h>
#include <stdint.h>

typedef __attribute__((ext_vector_type(8))) __bf16 bf16x8;
typedef __attribute__((ext_vector_type(4))) float f32x4;
typedef __attribute__((ext_vector_type(4))) unsigned u32x4;

#include <cstdio>
#include <vector>
#include <algorithm>
constexpr int NST = 12;
constexpr size_t ST_OFF = (size_t)128 << 20;
#define STAMP(i) do { __builtin_amdgcn_sched_barrier(0); st_[i] = __builtin_amdgcn_s_memrealtime(); __builtin_amdgcn_sched_barrier(0); } while (0)
#define STAMP_DECL unsigned long long st_[NST] = {}
#define STAMP_FLUSH(ws, slot) do { if ((threadIdx.x & 63) == 0) { unsigned long long* p_ = (unsigned long long*)((char*)(ws) + ST_OFF) + (size_t)(slot) * NST; for (int i_ = 0; i_ < NST; ++i_) p_[i_] = st_[i_]; } } while (0)
static void dump_stamps(void* ws) {
  const int n = 4096;
  std::vector<unsigned long long> h((size_t)n * NST);
  (void)hipMemcpy(h.data(), (char*)ws + ST_OFF, h.size() * 8, hipMemcpyDeviceToHost);
  unsigned long long base = ~0ull;
  for (int w = 0; w < 2048; ++w) base = std::min(base, h[(size_t)w * NST]);
  for (int grp = 0; grp < 2; ++grp)
    for (int i = 0; i < NST; ++i) {
      std::vector<double> v;
      for (int w = grp * 2048; w < grp * 2048 + (grp ? 256 : 2048); ++w) { unsigned long long t = h[(size_t)w * NST + i]; if (t >= base && t - base < 100000) v.push_back((t - base) * 0.01); }
      if (v.empty()) continue;
      std::sort(v.begin(), v.end());
      printf("  k%d st%-2d n=%4zu min %6.2f p10 %6.2f med %6.2f p90 %6.2f max %6.2f\n", grp, i, v.size(), v[0], v[v.size() / 10], v[v.size() / 2], v[v.size() * 9 / 10], v.back());
    }
}

namespace {
constexpr int NB = 8, NH = 32, NKV = 8, HD = 128, NPG = 256, PS = 16, LK = NPG * PS;
constexpr int WGH = 32;
constexpr int NW = 8;
constexpr int NP = 4;
constexpr int OST = 132;
constexpr float SCALE = 0.08838834764831845f;
constexpr float C2 = SCALE * 1.4426950408889634f;
constexpr float MASKV = -998244352.0f;
#define KV_AUX 2

constexpr int L_BT = 0;
constexpr int L_FIRST = L_BT + NB * NPG;
constexpr int L_M = L_FIRST + 8;
constexpr int L_L = L_M + 2 * NW * 32;
constexpr int L_O = L_L + NW * 32;
constexpr int L_END = L_O + NW * 32 * OST;

constexpr size_t W_ML = 0;
constexpr size_t W_O = W_ML + (size_t)NKV * WGH * 32 * 2;

__device__ __forceinline__ float xg_max(float x) {
  auto s = __builtin_amdgcn_permlane16_swap(__float_as_uint(x), __float_as_uint(x), false, false);
  x = fmaxf(__uint_as_float(s[0]), __uint_as_float(s[1]));
  auto t = __builtin_amdgcn_permlane32_swap(__float_as_uint(x), __float_as_uint(x), false, false);
  return fmaxf(__uint_as_float(t[0]), __uint_as_float(t[1]));
}
__device__ __forceinline__ float xg_sum(float x) {
  auto s = __builtin_amdgcn_permlane16_swap(__float_as_uint(x), __float_as_uint(x), false, false);
  x = __uint_as_float(s[0]) + __uint_as_float(s[1]);
  auto t = __builtin_amdgcn_permlane32_swap(__float_as_uint(x), __float_as_uint(x), false, false);
  return __uint_as_float(t[0]) + __uint_as_float(t[1]);
}
__device__ __forceinline__ unsigned pk_bf16(float lo, float hi) {
  typedef __attribute__((ext_vector_type(2))) __bf16 bf16x2;
  bf16x2 v = {(__bf16)lo, (__bf16)hi};
  return __builtin_bit_cast(unsigned, v);
}
__device__ __forceinline__ f32x4 mfma(u32x4 a, u32x4 b, f32x4 c) {
  return __builtin_amdgcn_mfma_f32_16x16x32_bf16(__builtin_bit_cast(bf16x8, a), __builtin_bit_cast(bf16x8, b), c, 0, 0, 0);
}

__global__ __launch_bounds__(NW * 64, 2) void attn_split(const unsigned short* __restrict__ q, const unsigned short* __restrict__ kc,
                                                         const unsigned short* __restrict__ vc, const int* __restrict__ bt,
                                                         const int* __restrict__ lens, float* __restrict__ ws, unsigned kv_bytes) {
  __shared__ __attribute__((aligned(16))) float lds[L_END];
  int* ldsi = reinterpret_cast<int*>(lds);
  STAMP_DECL;
  STAMP(0);
  const int tid = threadIdx.x, lane = tid & 63, wave = __builtin_amdgcn_readfirstlane(tid >> 6);
  const int g = lane >> 4, c = lane & 15;
  const int kvh = blockIdx.x & (NKV - 1), wi = blockIdx.x >> 3;

  const int col = tid & (NPG - 1), rb = (tid >> 8) * 4;
  const int t0 = bt[col];
  int tv[4];
#pragma unroll
  for (int i = 0; i < 4; ++i) tv[i] = bt[(rb + i) * NPG + col];
  u32x4 qf[2][4];
#pragma unroll
  for (int t = 0; t < 2; ++t) {
    const int row = 16 * t + c;
    const unsigned short* qp = q + ((row >> 2) * NH + kvh * 4 + (row & 3)) * HD + 8 * g;
#pragma unroll
    for (int s = 0; s < 4; ++s) qf[t][s] = *reinterpret_cast<const u32x4*>(qp + 32 * s);
  }
  int len[NB];
#pragma unroll
  for (int b = 0; b < NB; ++b) len[b] = min(max(lens[b], 0), LK);
  {
    const bool ne = (tv[0] != t0) | (tv[1] != t0) | (tv[2] != t0) | (tv[3] != t0);
    const unsigned long long m = __ballot(ne);
#pragma unroll
    for (int i = 0; i < 4; ++i) ldsi[L_BT + (rb + i) * NPG + col] = tv[i];
    if (lane == 0) ldsi[L_FIRST + wave] = m ? (wave & 3) * 64 + __builtin_ctzll(m) : NPG;
  }
  STAMP(1);
  __syncthreads();
  STAMP(2);
  int S = NPG;
#pragma unroll
  for (int w = 0; w < NW; ++w) S = min(S, ldsi[L_FIRST + w]);
#pragma unroll
  for (int b = 0; b < NB; ++b) S = min(S, len[b] >> 4);
  S = __builtin_amdgcn_readfirstlane(S);
  int off[NB + 1];
  off[0] = S;
#pragma unroll
  for (int b = 0; b < NB; ++b) off[b + 1] = off[b] + ((len[b] + 15) >> 4) - S;
  const int U = off[NB];
  const int u0 = (wi * U) / WGH, u1 = ((wi + 1) * U) / WGH;

  const __amdgpu_buffer_rsrc_t krs = __builtin_amdgcn_make_buffer_rsrc((void*)(kc + kvh * HD), 0, kv_bytes - kvh * HD * 2, 0x00020000);
  const __amdgpu_buffer_rsrc_t vrs = __builtin_amdgcn_make_buffer_rsrc((void*)(vc + kvh * HD), 0, kv_bytes - kvh * HD * 2, 0x00020000);
  const int koff = (4 * (c & 3) + (c >> 2)) * (NKV * HD * 2) + g * 16;
  const int voff = g * (NKV * HD * 2) + c * 16;

  int rlo = 0, rhi = 32;
  if (u0 >= S) {
    int blo = 0, bhi = 0;
#pragma unroll
    for (int b = 1; b < NB; ++b) {
      if (u0 >= off[b]) blo = b;
      if (u1 - 1 >= off[b]) bhi = b;
    }
    rlo = 4 * blo;
    rhi = 4 * bhi + 4;
  }
  if (u1 <= u0) rhi = rlo = 0;
  float M[2] = {-INFINITY, -INFINITY}, l[2] = {0.f, 0.f};

  unsigned so_v;
  int info_v;
  {
    const int u = u0 + wave + NW * lane;
    int bsel = 0, base = off[0], lb = len[0];
#pragma unroll
    for (int b = 1; b < NB; ++b)
      if (u >= off[b]) { bsel = b; base = off[b]; lb = len[b]; }
    const bool sh = u < S;
    const int j = min(sh ? u : S + u - base, NPG - 1);
    const int phys = ldsi[L_BT + (sh ? 0 : bsel) * NPG + j];
    so_v = (u < u1) ? (unsigned)phys * (PS * NKV * HD * 2) : 0x80000000u;
    info_v = (u < u1) ? ((sh ? 16 : min(16, lb - 16 * j)) | ((sh ? 8 : bsel) << 8)) : 0;
  }
  const int nrd = (u1 - u0 + NW * NP - 1) / (NW * NP);
  for (int rd = 0; rd < nrd; ++rd) {
    int par = rd;
    asm volatile("" : "+s"(par));
    unsigned so[NP];
    int info[NP];
#pragma unroll
    for (int p = 0; p < NP; ++p) {
      so[p] = __builtin_amdgcn_readlane(so_v, NP * par + p);
      info[p] = __builtin_amdgcn_readlane(info_v, NP * par + p);
    }
    u32x4 kf[NP][4], vf[NP][4];
    STAMP(3);
#pragma unroll
    for (int p = 0; p < NP; ++p)
#pragma unroll
      for (int s = 0; s < 4; ++s) kf[p][s] = __builtin_amdgcn_raw_buffer_load_b128(krs, koff + 64 * s, so[p], KV_AUX);
#pragma unroll
    for (int p = 0; p < NP; ++p)
#pragma unroll
      for (int i = 0; i < 4; ++i) vf[p][i] = __builtin_amdgcn_raw_buffer_load_b128(vrs, voff, so[p] + i * (4 * NKV * HD * 2), KV_AUX);
    __builtin_amdgcn_sched_barrier(0);
    STAMP(4);

    f32x4 sc[NP][2];
    float mx[2] = {-INFINITY, -INFINITY};
#pragma unroll
    for (int p = 0; p < NP; ++p) {
      const int nv = info[p] & 255, sq = info[p] >> 8;
#pragma unroll
      for (int t = 0; t < 2; ++t) {
        f32x4 a = {0.f, 0.f, 0.f, 0.f};
#pragma unroll
        for (int s = 0; s < 4; ++s) a = mfma(kf[p][s], qf[t][s], a);
        const bool cv = (sq == 8) | (sq == 4 * t + (c >> 2));
#pragma unroll
        for (int r = 0; r < 4; ++r) {
          a[r] = (cv & (4 * r + g < nv)) ? a[r] : -INFINITY;
          mx[t] = fmaxf(mx[t], a[r]);
        }
        sc[p][t] = a;
      }
    }
#pragma unroll
    for (int t = 0; t < 2; ++t) mx[t] = xg_max(mx[t]);
    if (g < 2) lds[L_M + (par * NW + wave) * 32 + 16 * g + c] = g ? mx[1] : mx[0];
    STAMP(5);
    __syncthreads();
    STAMP(6);
    u32x4 pf[NP / 2][2];
    float al[2];
#pragma unroll
    for (int t = 0; t < 2; ++t) {
      float mn = M[t];
#pragma unroll
      for (int w = 0; w < NW; ++w) mn = fmaxf(mn, lds[L_M + (par * NW + w) * 32 + 16 * t + c]);
      const float ms = (mn == -INFINITY) ? 0.f : mn * C2;
      const float alpha = __builtin_amdgcn_exp2f(M[t] * C2 - ms);
      M[t] = mn;
      float ls = l[t] * alpha;
      al[t] = alpha;
#pragma unroll
      for (int p = 0; p < NP; ++p) {
        float e[4];
#pragma unroll
        for (int r = 0; r < 4; ++r) { e[r] = __builtin_amdgcn_exp2f(fmaf(sc[p][t][r], C2, -ms)); ls += e[r]; }
        pf[p >> 1][t][(p & 1) * 2] = pk_bf16(e[0], e[1]);
        pf[p >> 1][t][(p & 1) * 2 + 1] = pk_bf16(e[2], e[3]);
      }
      l[t] = ls;
    }
    f32x4 acc[2][8];
#pragma unroll
    for (int t = 0; t < 2; ++t)
#pragma unroll
      for (int d = 0; d < 8; ++d) acc[t][d] = f32x4{0.f, 0.f, 0.f, 0.f};
#pragma unroll
    for (int pp = 0; pp < NP / 2; ++pp) {
#pragma unroll
      for (int e = 0; e < 4; ++e) {
        u32x4 lo, hi;
#pragma unroll
        for (int h = 0; h < 2; ++h) {
          const u32x4* v = vf[2 * pp + h];
          lo[2 * h] = __builtin_amdgcn_perm(v[1][e], v[0][e], 0x05040100u);
          lo[2 * h + 1] = __builtin_amdgcn_perm(v[3][e], v[2][e], 0x05040100u);
          hi[2 * h] = __builtin_amdgcn_perm(v[1][e], v[0][e], 0x07060302u);
          hi[2 * h + 1] = __builtin_amdgcn_perm(v[3][e], v[2][e], 0x07060302u);
        }
#pragma unroll
        for (int t = 0; t < 2; ++t) {
          acc[t][2 * e] = mfma(lo, pf[pp][t], acc[t][2 * e]);
          acc[t][2 * e + 1] = mfma(hi, pf[pp][t], acc[t][2 * e + 1]);
        }
      }
    }
    STAMP(7);
#pragma unroll
    for (int t = 0; t < 2; ++t) {
      const int row = 16 * t + c;
      if (row >= rlo && row < rhi) {
        f32x4* op = reinterpret_cast<f32x4*>(&lds[L_O + (wave * 32 + row) * OST + 32 * g]);
        if (par != 0) {
#pragma unroll
          for (int d = 0; d < 8; ++d) acc[t][d] += op[d] * al[t];
        }
#pragma unroll
        for (int d = 0; d < 8; ++d) op[d] = acc[t][d];
      }
    }
  }

#pragma unroll
  for (int t = 0; t < 2; ++t) {
    const float ls = xg_sum(l[t]);
    const int row = 16 * t + c;
    if (g == 0) lds[L_L + wave * 32 + row] = ls;
  }
  STAMP(8);
  __syncthreads();
  STAMP(9);
  const size_t slot = (size_t)(kvh * WGH + wi) * 32;
  for (int it = tid; it < (rhi - rlo) * 32; it += NW * 64) {
    const int row = rlo + (it >> 5), x = it & 31;
    f32x4 o = {0.f, 0.f, 0.f, 0.f};
#pragma unroll
    for (int w = 0; w < NW; ++w) o += *reinterpret_cast<const f32x4*>(&lds[L_O + (w * 32 + row) * OST + 4 * x]);
    *reinterpret_cast<f32x4*>(ws + W_O + (slot + row) * HD + 4 * x) = o;
  }
  if (tid < 32) {
    float L = 0.f;
#pragma unroll
    for (int w = 0; w < NW; ++w) L += lds[L_L + w * 32 + tid];
    const bool ok = tid >= rlo && tid < rhi;
    *reinterpret_cast<float2*>(ws + W_ML + (slot + tid) * 2) = make_float2(g ? M[1] : M[0], ok ? L : 0.f);
  }
  STAMP(10);
  asm volatile("s_waitcnt vmcnt(0)" ::: "memory");
  STAMP(11);
  STAMP_FLUSH(ws, blockIdx.x * NW + wave);
}

__global__ __launch_bounds__(64) void attn_combine(const unsigned short* __restrict__ vc, const int* __restrict__ bt, const int* __restrict__ lens,
                                                   const float* __restrict__ ws, unsigned short* __restrict__ out) {
  const int lane = threadIdx.x, kvh = blockIdx.x & (NKV - 1), row = blockIdx.x >> 3, b = row >> 2;
  const int hw = lane >> 5, x = lane & 31;
  STAMP_DECL;
  STAMP(0);
  const size_t slot0 = (size_t)kvh * WGH * 32 + row;
  const float2 ml = *reinterpret_cast<const float2*>(ws + W_ML + (slot0 + (size_t)x * 32) * 2);
  const bool ok = ml.y > 0.f;
  STAMP(1);
  float M = ok ? ml.x : -INFINITY;
#pragma unroll
  for (int s = 1; s < 32; s <<= 1) M = fmaxf(M, __shfl_xor(M, s));
  const float wgt = ok ? __builtin_amdgcn_exp2f((ml.x - M) * C2) : 0.f;
  float L = wgt * ml.y;
#pragma unroll
  for (int s = 1; s < 32; s <<= 1) L += __shfl_xor(L, s);
  const __amdgpu_buffer_rsrc_t ors = __builtin_amdgcn_make_buffer_rsrc((void*)(ws + W_O + slot0 * HD), 0, (unsigned)(((WGH - 1) * 32 + 1) * HD * 4), 0x00020000);
  const unsigned long long okm = __ballot(ok);
  u32x4 ov[WGH / 2];
#pragma unroll
  for (int k = 0; k < WGH / 2; ++k) {
    const int i = 2 * k + hw;
    const unsigned vo = ((okm >> i) & 1) ? (unsigned)(i * 32 * HD * 4 + x * 16) : 0x80000000u;
    ov[k] = __builtin_amdgcn_raw_buffer_load_b128(ors, vo, 0, 0);
  }
  f32x4 o = {0.f, 0.f, 0.f, 0.f};
#pragma unroll
  for (int k = 0; k < WGH / 2; ++k) {
    const float w = __shfl(wgt, 2 * k + hw);
    o += w * __builtin_bit_cast(f32x4, ov[k]);
  }
#pragma unroll
  for (int r = 0; r < 4; ++r) o[r] += __shfl_xor(o[r], 32);
  STAMP(2);
  const int d0 = 32 * (x >> 3) + (x & 7);
  const int len = min(max(lens[b], 0), LK);
  if (len < LK && !(M * SCALE >= MASKV + 256.f)) {
    const float Mn = M * SCALE, Mx = fmaxf(Mn, MASKV);
    const float wv = (L > 0.f) ? __expf(Mn - Mx) : 0.f, wm = __expf(MASKV - Mx);
    f32x4 sv = {0.f, 0.f, 0.f, 0.f};
    for (int pos = len; pos < LK; ++pos) {
      const unsigned short* vp = vc + ((size_t)bt[b * NPG + (pos >> 4)] * PS + (pos & 15)) * (NKV * HD) + kvh * HD + d0;
#pragma unroll
      for (int r = 0; r < 4; ++r) sv[r] += __uint_as_float((unsigned)vp[8 * r] << 16);
    }
    o = o * wv + sv * wm;
    L = L * wv + wm * (float)(LK - len);
  }
  const float inv = 1.f / L;
  if (hw == 0) {
    unsigned short* op = out + (b * NH + kvh * 4 + (row & 3)) * HD + d0;
#pragma unroll
    for (int r = 0; r < 4; ++r) op[8 * r] = __builtin_bit_cast(unsigned short, (__bf16)(o[r] * inv));
  }
  STAMP(3);
  asm volatile("s_waitcnt vmcnt(0)" ::: "memory");
  STAMP(4);
  STAMP_FLUSH(ws, 2048 + blockIdx.x);
}

__global__ __launch_bounds__(256) void diag_k(const float* ws) {
  __shared__ unsigned long long sm[2][256];
  const unsigned long long* p = (const unsigned long long*)((const char*)ws + ST_OFF);
  unsigned long long mn = ~0ull, mx = DIAG_MIN ? ~0ull : 0;
  for (int w = threadIdx.x; w < 2048; w += 256) mn = min(mn, p[(size_t)w * NST]);
  const int lo = DIAG_K ? 2048 : 0, n = DIAG_K ? DIAG_N1 : 2048;
  for (int w = threadIdx.x; w < n; w += 256) { unsigned long long t = p[(size_t)(lo + w) * NST + DIAG_I]; mx = DIAG_MIN ? min(mx, t) : max(mx, t); }
  sm[0][threadIdx.x] = mn; sm[1][threadIdx.x] = mx;
  __syncthreads();
  if (threadIdx.x == 0) {
    for (int i = 0; i < 256; ++i) { mn = min(mn, sm[0][i]); mx = DIAG_MIN ? min(mx, sm[1][i]) : max(mx, sm[1][i]); }
    unsigned long long q = (mx - mn + 5) / 10;
    if (q > 400) q = 400;
    const unsigned long long target = mn + 10000 + 800 * q;
    for (int it = 0; it < 1000000; ++it) { if (__builtin_amdgcn_s_memrealtime() >= target) break; __builtin_amdgcn_s_sleep(1); }
  }
}
}

extern "C" void kernel_launch(void* const* d_in, const int* in_sizes, int n_in, void* d_out, int out_size, void* d_ws, size_t ws_size,
                              hipStream_t stream) {
  const unsigned short* q = (const unsigned short*)d_in[0];
  const unsigned short* kc = (const unsigned short*)d_in[1];
  const unsigned short* vc = (const unsigned short*)d_in[2];
  const int* bt = (const int*)d_in[3];
  const int* lens = (const int*)d_in[4];
  const unsigned kv_bytes = (unsigned)in_sizes[1] * 2u;
  attn_split<<<NKV * WGH, NW * 64, 0, stream>>>(q, kc, vc, bt, lens, (float*)d_ws, kv_bytes);
  attn_combine<<<NKV * 32, 64, 0, stream>>>(vc, bt, lens, (const float*)d_ws, (unsigned short*)d_out);
  diag_k<<<1, 256, 0, stream>>>((const float*)d_ws);
}
```

```cpp
#include <hip/hip_runtime.h>
#include <hip/hip_bf16.h>
#include <stdint.h>
#include <type_traits>

typedef __attribute__((ext_vector_type(8))) __bf16 bf16x8;
typedef __attribute__((ext_vector_type(4))) float f32x4;
typedef __attribute__((ext_vector_type(2))) float f32x2;
typedef __attribute__((ext_vector_type(4))) unsigned u32x4;
typedef __attribute__((ext_vector_type(2))) unsigned u32x2;
typedef unsigned long long u64;

#define STAMP(i)
#define STAMP_DECL
#define STAMP_FLUSH(ws, slot)

namespace {
constexpr int NB = 8, NH = 32, NKV = 8, HD = 128, NPG = 256, PS = 16, LK = NPG * PS;
constexpr int WGH = 32;
constexpr int NW = 8;
constexpr int NP = 4;
constexpr float SCALE = 0.08838834764831845f;
constexpr float C2 = SCALE * 1.4426950408889634f;
constexpr float MASKV = -998244352.0f;
constexpr unsigned TOKB = NKV * HD * 2, PAGEB = PS * TOKB;
constexpr unsigned OOB = 0x80000000u;
constexpr unsigned SENT = 0xFFFFFFFFu;
constexpr unsigned SPIN = 1u << 18;

constexpr int PIECE = 1040, KPAGE = 4 * PIECE, OROW = 528, WREG = 32 * OROW;
constexpr int L_Q = NW * WREG, L_BT = L_Q + NB * PIECE, L_ML = L_BT + NB * NPG * 4, L_FIRST = L_ML + NW * 32 * 8, L_CNT = L_FIRST + 32, L_END = L_CNT + 16;
static_assert(NP * KPAGE <= WREG && L_END <= 160 * 1024, "lds");

constexpr unsigned W_EPOCH = 0;
constexpr unsigned W_FLAG = NKV * 128;
constexpr unsigned W_O = W_FLAG + NKV * 32 * 128;
constexpr unsigned W_END = W_O + NKV * 32 * WGH * HD * 4;

__device__ __forceinline__ float xg_max(float x) {
  auto s = __builtin_amdgcn_permlane16_swap(__float_as_uint(x), __float_as_uint(x), false, false);
  x = fmaxf(__uint_as_float(s[0]), __uint_as_float(s[1]));
  auto t = __builtin_amdgcn_permlane32_swap(__float_as_uint(x), __float_as_uint(x), false, false);
  return fmaxf(__uint_as_float(t[0]), __uint_as_float(t[1]));
}
__device__ __forceinline__ float xg_sum(float x) {
  auto s = __builtin_amdgcn_permlane16_swap(__float_as_uint(x), __float_as_uint(x), false, false);
  x = __uint_as_float(s[0]) + __uint_as_float(s[1]);
  auto t = __builtin_amdgcn_permlane32_swap(__float_as_uint(x), __float_as_uint(x), false, false);
  return __uint_as_float(t[0]) + __uint_as_float(t[1]);
}
template <int CTRL> __device__ __forceinline__ float dpp(float x) {
  return __builtin_bit_cast(float, __builtin_amdgcn_mov_dpp(__builtin_bit_cast(int, x), CTRL, 0xf, 0xf, true));
}
template <int CTRL> __device__ __forceinline__ int dppi(int x) { return __builtin_amdgcn_mov_dpp(x, CTRL, 0xf, 0xf, true); }
__device__ __forceinline__ int min8(int x) { x = min(x, dppi<0xB1>(x)); x = min(x, dppi<0x4E>(x)); return min(x, dppi<0x141>(x)); }
__device__ __forceinline__ int max8(int x) { x = max(x, dppi<0xB1>(x)); x = max(x, dppi<0x4E>(x)); return max(x, dppi<0x141>(x)); }
__device__ __forceinline__ unsigned min8u(unsigned x) {
  x = min(x, (unsigned)dppi<0xB1>((int)x)); x = min(x, (unsigned)dppi<0x4E>((int)x)); return min(x, (unsigned)dppi<0x141>((int)x));
}
__device__ __forceinline__ float row_max(float x) {
  x = fmaxf(x, dpp<0xB1>(x)); x = fmaxf(x, dpp<0x4E>(x)); x = fmaxf(x, dpp<0x141>(x)); return fmaxf(x, dpp<0x128>(x));
}
__device__ __forceinline__ float row_sum(float x) {
  x += dpp<0xB1>(x); x += dpp<0x4E>(x); x += dpp<0x141>(x); return x + dpp<0x128>(x);
}
__device__ __forceinline__ unsigned pk_bf16(float lo, float hi) {
  typedef __attribute__((ext_vector_type(2))) __bf16 bf16x2;
  bf16x2 v = {(__bf16)lo, (__bf16)hi};
  return __builtin_bit_cast(unsigned, v);
}
__device__ __forceinline__ f32x4 mfma(u32x4 a, u32x4 b, f32x4 c) {
  return __builtin_amdgcn_mfma_f32_16x16x32_bf16(__builtin_bit_cast(bf16x8, a), __builtin_bit_cast(bf16x8, b), c, 0, 0, 0);
}
__device__ __forceinline__ unsigned canon(unsigned u) { return u == SENT ? 0x7FFFFFFFu : u; }
template <int N> __device__ __forceinline__ void wait_vm() { asm volatile("s_waitcnt vmcnt(%0)" ::"n"(N) : "memory"); }
template <int LO, int SO> __device__ __forceinline__ void dma_nt(unsigned lds, unsigned voff, __amdgpu_buffer_rsrc_t rs, unsigned soff) {
  unsigned t;
  asm volatile("s_add_u32 m0, %1, %5\n\ts_add_u32 %0, %4, %6\n\tbuffer_load_dwordx4 %2, %3, %0 offen nt lds"
               : "=&s"(t) : "s"(lds), "v"(voff), "s"(rs), "s"(soff), "n"(LO), "n"(SO) : "memory", "scc");
}
template <int P> __device__ __forceinline__ void dma_page(unsigned lds, unsigned voff, __amdgpu_buffer_rsrc_t rs, unsigned soff) {
  dma_nt<P * KPAGE, 0>(lds, voff, rs, soff);
  dma_nt<P * KPAGE + PIECE, 4 * TOKB>(lds, voff, rs, soff);
  dma_nt<P * KPAGE + 2 * PIECE, 8 * TOKB>(lds, voff, rs, soff);
  dma_nt<P * KPAGE + 3 * PIECE, 12 * TOKB>(lds, voff, rs, soff);
}
__device__ __forceinline__ void dma(unsigned lds, unsigned voff, __amdgpu_buffer_rsrc_t rs, unsigned soff) {
  asm volatile("s_mov_b32 m0, %0\n\ts_nop 0\n\tbuffer_load_dwordx4 %1, %2, %3 offen lds" ::"s"(lds), "v"(voff), "s"(rs), "s"(soff) : "memory");
}

__global__ __launch_bounds__(NW * 64, 2) void attn_kernel(const unsigned short* __restrict__ q, const unsigned short* __restrict__ kc,
                                                          const unsigned short* __restrict__ vc, const int* __restrict__ bt,
                                                          const int* __restrict__ lens, unsigned short* __restrict__ out,
                                                          unsigned char* __restrict__ ws, unsigned kv_bytes) {
  __shared__ __attribute__((aligned(16))) unsigned char lds[L_END];
  STAMP_DECL;
  STAMP(0);
  const int tid = threadIdx.x, lane = tid & 63, wave = __builtin_amdgcn_readfirstlane(tid >> 6);
  const int g = lane >> 4, c = lane & 15;
  const int kvh = blockIdx.x & (NKV - 1), wi = blockIdx.x >> 3;
  const unsigned ldsb = (unsigned)(size_t)lds;
  asm volatile("" ::"s"(q), "s"(kc), "s"(vc), "s"(bt), "s"(lens), "s"(out), "s"(ws), "s"(kv_bytes));

  const int sb = lane & 7;
  const u32x4 tv = *reinterpret_cast<const u32x4*>(bt + wave * NPG + 4 * lane);
  const u32x4 t0 = *reinterpret_cast<const u32x4*>(bt + 4 * lane);
  int len = lens[sb];
  const u64 tag = *(const __attribute__((address_space(4))) u64*)(ws + W_EPOCH + kvh * 128) + 1;
  __builtin_amdgcn_sched_barrier(0);
  const unsigned swz = (unsigned)(c ^ ((g & 2) << 2)) * 16;
  {
    const __amdgpu_buffer_rsrc_t qrs = __builtin_amdgcn_make_buffer_rsrc((void*)q, 0, NB * NH * HD * 2, 0x00020000);
    dma(ldsb + L_Q + wave * PIECE, g * 256 + swz, qrs, (wave * NH + kvh * 4) * HD * 2);
  }
  const __amdgpu_buffer_rsrc_t krs = __builtin_amdgcn_make_buffer_rsrc((void*)(kc + kvh * HD), 0, kv_bytes - kvh * HD * 2, 0x00020000);
  const __amdgpu_buffer_rsrc_t vrs = __builtin_amdgcn_make_buffer_rsrc((void*)(vc + kvh * HD), 0, kv_bytes - kvh * HD * 2, 0x00020000);
  const __amdgpu_buffer_rsrc_t wrs = __builtin_amdgcn_make_buffer_rsrc((void*)ws, 0, W_END, 0x00020000);
  const unsigned rowslot = (unsigned)(kvh * 32 + wi) * WGH;
  if (tid == 0) *reinterpret_cast<int*>(lds + L_CNT) = 0;
  unsigned voff = g * TOKB + c * 16, koff = g * TOKB + swz;
  const unsigned wreg = wave * WREG;
  unsigned krd = wreg + (c & 3) * PIECE + (c >> 2) * 256 + ((4 * g) ^ (c & 8)) * 16;
  unsigned qrd = L_Q + (c & 3) * PIECE + (c >> 2) * 256 + ((4 * g) ^ (c & 8)) * 16;
  f32x4 acc[2][8];
#pragma unroll
  for (int t = 0; t < 2; ++t)
#pragma unroll
    for (int d = 0; d < 8; ++d) acc[t][d] = f32x4{0.f, 0.f, 0.f, 0.f};
  float M[2] = {-INFINITY, -INFINITY}, l[2] = {0.f, 0.f};
  unsigned kdst = ldsb + wreg;
  asm volatile("" : "+v"(voff), "+v"(koff), "+v"(krd), "+v"(qrd), "+s"(kdst) : "s"(krs), "s"(vrs), "s"(wrs));
  __builtin_amdgcn_sched_barrier(0);
  len = min(max(len, 0), LK);
  const int np = (len + 15) >> 4;
  int psn = np;
  { const int t = dppi<0x111>(psn); psn += sb >= 1 ? t : 0; }
  { const int t = dppi<0x112>(psn); psn += sb >= 2 ? t : 0; }
  { const int t = dppi<0x114>(psn); psn += sb >= 4 ? t : 0; }
  int ps8 = __builtin_amdgcn_readlane(psn, 7);
  {
    int first = min8(len >> 4);
#pragma unroll
    for (int k = 0; k < 4; ++k) {
      const u64 m = __ballot(tv[k] != t0[k]);
      first = min(first, m ? 4 * (int)__builtin_ctzll(m) + k : NPG);
    }
    *reinterpret_cast<u32x4*>(lds + L_BT + wave * (NPG * 4) + lane * 16) = tv;
    if (lane == 0) *reinterpret_cast<int*>(lds + L_FIRST + wave * 4) = first;
  }
  int uo = wave + (lane & 56), bta = L_BT + sb * (NPG * 4);
  asm volatile("" : "+v"(psn), "+s"(ps8), "+v"(uo), "+v"(bta));
  STAMP(1);
  asm volatile("s_waitcnt vmcnt(0) lgkmcnt(0)\n\ts_barrier" ::: "memory");
  STAMP(2);
  int S;
  {
    const u32x4 a = *reinterpret_cast<const u32x4*>(lds + L_FIRST), b = *reinterpret_cast<const u32x4*>(lds + L_FIRST + 16);
    S = __builtin_amdgcn_readfirstlane((int)min(min(min(a[0], a[1]), min(a[2], a[3])), min(min(b[0], b[1]), min(b[2], b[3]))));
  }
  const int offn = psn - sb * S, offb = offn - np + S;
  const int U = ps8 - (NB - 1) * S;
  const int u0 = (wi * U) >> 5, u1 = ((wi + 1) * U) >> 5;
  unsigned so_v;
  int info_v;
  {
    const int u = u0 + uo;
    const bool sh = u < S;
    const bool hit = (u < u1) & (sh ? sb == 0 : (u >= offb) & (u < offn));
    const int j = sh ? u : min(max(S + u - offb, 0), NPG - 1);
    const int phys = *reinterpret_cast<const int*>(lds + bta + j * 4);
    so_v = (unsigned)min8u(hit ? (unsigned)phys * PAGEB : OOB);
    info_v = hit ? (sh ? 16 | 8 << 8 : min(16, len - 16 * j) | sb << 8) : 0;
  }
  const int cb = (wi >> 4) * 4 + (wi & 3), ch = (wi & 15) >> 2;

  u32x2 fl;
  unsigned flo;
  const u32x4 tag4 = {(unsigned)tag, (unsigned)(tag >> 32), (unsigned)tag, (unsigned)(tag >> 32)};
  int blo, nseq, clen;
  unsigned pmask;

  auto round = [&](auto rdc) __attribute__((always_inline)) {
    constexpr int rd = decltype(rdc)::value;
    unsigned so[NP];
#pragma unroll
    for (int p = 0; p < NP; ++p) so[p] = __builtin_amdgcn_readlane(so_v, 8 * (NP * rd + p));
    STAMP(3);
    __builtin_amdgcn_s_setprio(1);
    dma_page<0>(kdst, koff, krs, so[0]);
    __builtin_amdgcn_sched_barrier(0);
    if constexpr (rd == 0) {
      const int cofl = __builtin_amdgcn_readlane(offb, cb), cofh = __builtin_amdgcn_readlane(offn, cb);
      const int i = lane & 31, a0 = (i * U) >> 5, a1 = ((i + 1) * U) >> 5;
      pmask = (unsigned)__ballot((a1 > a0) & ((a0 < S) | ((a0 < cofh) & (a1 > cofl))));
      const u32x4 sv = {SENT, SENT, SENT, SENT};
      const unsigned pm = pmask >> (4 * wave + (lane >> 5));
      __builtin_amdgcn_raw_buffer_store_b128(sv, wrs, (pm & 1) ? lane * 16 : OOB, W_O + (rowslot + 4 * wave) * (HD * 4), 16);
      __builtin_amdgcn_raw_buffer_store_b128(sv, wrs, (pm & 4) ? lane * 16 + 1024 : OOB, W_O + (rowslot + 4 * wave) * (HD * 4), 16);
    }
    __builtin_amdgcn_sched_barrier(0);
    dma_page<1>(kdst, koff, krs, so[1]);
    dma_page<2>(kdst, koff, krs, so[2]);
    dma_page<3>(kdst, koff, krs, so[3]);
    __builtin_amdgcn_s_setprio(0);
    __builtin_amdgcn_sched_barrier(0);
    STAMP(4);
    int info[NP];
    {
      const int inf = max8(info_v);
#pragma unroll
      for (int p = 0; p < NP; ++p) info[p] = __builtin_amdgcn_readlane(inf, 8 * (NP * rd + p));
    }
    if constexpr (rd == 0) {
      const unsigned sm = (unsigned)__ballot((u1 > u0) & ((u0 < S) | ((u0 < offn) & (u1 > offb)))) & 255u;
      blo = sm ? __builtin_ctz(sm) : 0;
      nseq = __builtin_popcount(sm);
      clen = __builtin_amdgcn_readlane(len, cb);
      asm volatile("" : "+s"(blo), "+s"(nseq), "+s"(clen), "+s"(pmask));
      const bool need = (wave == NW - 1) & (lane < 32) & ((wi == 0) | ((unsigned)((lane >> 4) * 4 + (lane & 3) - blo) < (unsigned)nseq));
      flo = need ? W_FLAG + (kvh * 32 + lane) * 128 : OOB;
    }
    u32x4 qf[2][4];
#pragma unroll
    for (int t = 0; t < 2; ++t)
#pragma unroll
      for (int s = 0; s < 4; ++s) qf[t][s] = *reinterpret_cast<const u32x4*>(lds + qrd + t * 4 * PIECE + s * 16);

    u32x4 vf[NP][4];
    f32x4 sc[NP][2];
    float mx[2] = {M[0], M[1]};
#pragma unroll
    for (int p = 0; p < NP; ++p) {
      if (p == 0) wait_vm<rd == 0 ? 14 : 12>();
      if (p == 1) wait_vm<8>();
      if (p == 2) wait_vm<4>();
      if (p == 3) wait_vm<0>();
      if (p == 1 && rd == 0) {
        int old = 0;
        if (lane == 0) old = __hip_atomic_fetch_add(reinterpret_cast<int*>(lds + L_CNT), 1, __ATOMIC_RELAXED, __HIP_MEMORY_SCOPE_WORKGROUP);
        if (__builtin_amdgcn_readfirstlane(old) == NW - 1)
          __builtin_amdgcn_raw_buffer_store_b128(tag4, wrs, lane < 8 ? lane * 16 : OOB, W_FLAG + (kvh * 32 + wi) * 128, 16);
      }
      u32x4 kf[4];
#pragma unroll
      for (int s = 0; s < 4; ++s) kf[s] = *reinterpret_cast<const u32x4*>(lds + krd + p * KPAGE + s * 16);
      const int nv = info[p] & 255, sq = info[p] >> 8;
#pragma unroll
      for (int t = 0; t < 2; ++t) {
        f32x4 a = {0.f, 0.f, 0.f, 0.f};
#pragma unroll
        for (int s = 0; s < 4; ++s) a = mfma(kf[s], qf[t][s], a);
        const bool cv = (sq == 8) | (sq == 4 * t + (c & 3));
#pragma unroll
        for (int r = 0; r < 4; ++r) {
          a[r] = (cv & (4 * r + g < nv)) ? a[r] : -INFINITY;
          mx[t] = fmaxf(mx[t], a[r]);
        }
        sc[p][t] = a;
      }
    }
    __builtin_amdgcn_sched_barrier(0);
#pragma unroll
    for (int p = 0; p < NP; ++p)
#pragma unroll
      for (int i = 0; i < 4; ++i) vf[p][i] = __builtin_amdgcn_raw_buffer_load_b128(vrs, voff, so[p] + i * 4 * TOKB, 2);
    fl = __builtin_amdgcn_raw_buffer_load_b64(wrs, flo, 0, 16);
    STAMP(5);
    __builtin_amdgcn_sched_barrier(0);
    u32x4 pf[NP / 2][2];
#pragma unroll
    for (int t = 0; t < 2; ++t) {
      const float mn = xg_max(mx[t]);
      const float ms = (mn == -INFINITY) ? 0.f : mn * C2;
      float ls = 0.f;
      if constexpr (rd != 0) {
        const float alpha = __builtin_amdgcn_exp2f(M[t] * C2 - ms);
        ls = l[t] * alpha;
#pragma unroll
        for (int d = 0; d < 8; ++d) acc[t][d] *= alpha;
      }
      M[t] = mn;
#pragma unroll
      for (int p = 0; p < NP; ++p) {
        float e[4];
#pragma unroll
        for (int r = 0; r < 4; ++r) { e[r] = __builtin_amdgcn_exp2f(fmaf(sc[p][t][r], C2, -ms)); ls += e[r]; }
        pf[p >> 1][t][(p & 1) * 2] = pk_bf16(e[0], e[1]);
        pf[p >> 1][t][(p & 1) * 2 + 1] = pk_bf16(e[2], e[3]);
      }
      l[t] = ls;
    }
    STAMP(6);
    __builtin_amdgcn_sched_barrier(0);
#pragma unroll
    for (int pp = 0; pp < NP / 2; ++pp) {
#pragma unroll
      for (int e = 0; e < 4; ++e) {
        u32x4 lo, hi;
#pragma unroll
        for (int h = 0; h < 2; ++h) {
          const u32x4* v = vf[2 * pp + h];
          lo[2 * h] = __builtin_amdgcn_perm(v[1][e], v[0][e], 0x05040100u);
          lo[2 * h + 1] = __builtin_amdgcn_perm(v[3][e], v[2][e], 0x05040100u);
          hi[2 * h] = __builtin_amdgcn_perm(v[1][e], v[0][e], 0x07060302u);
          hi[2 * h + 1] = __builtin_amdgcn_perm(v[3][e], v[2][e], 0x07060302u);
        }
#pragma unroll
        for (int t = 0; t < 2; ++t) {
          acc[t][2 * e] = mfma(lo, pf[pp][t], acc[t][2 * e]);
          acc[t][2 * e + 1] = mfma(hi, pf[pp][t], acc[t][2 * e + 1]);
        }
      }
      __builtin_amdgcn_sched_barrier(0);
    }
  };
  round(std::integral_constant<int, 0>{});
  if (u1 - u0 > NW * NP) round(std::integral_constant<int, 1>{});
  STAMP(7);
#pragma unroll
  for (int t = 0; t < 2; ++t) {
    const float ls = xg_sum(l[t]);
    if (g == 0) *reinterpret_cast<f32x2*>(lds + L_ML + (wave * 32 + 16 * t + c) * 8) = f32x2{M[t], ls};
    if ((unsigned)(4 * t + (c & 3) - blo) < (unsigned)nseq) {
#pragma unroll
      for (int d = 0; d < 8; ++d) *reinterpret_cast<f32x4*>(lds + wreg + (16 * t + c) * OROW + g * 128 + d * 16) = acc[t][d];
    }
  }
#pragma nounroll
  for (unsigned it = 0; it < SPIN; ++it) {
    const bool ok = (flo == OOB) | ((fl[0] == (unsigned)tag) & (fl[1] == (unsigned)(tag >> 32)));
    if (__all(ok)) break;
    asm volatile("s_sleep 2" ::: "memory");
    fl = __builtin_amdgcn_raw_buffer_load_b64(wrs, flo, 0, 16);
  }
  STAMP(8);
  __syncthreads();
  STAMP(9);
  {
    const int x = tid & 31;
    for (int k = tid >> 5; k < 4 * nseq; k += NW * 2) {
      const int b = blo + (k >> 2), row = (b >> 2) * 16 + (k & 3) * 4 + (b & 3);
      f32x2 ml[NW];
      f32x4 ow[NW];
#pragma unroll
      for (int w = 0; w < NW; ++w) {
        ml[w] = *reinterpret_cast<const f32x2*>(lds + L_ML + (w * 32 + row) * 8);
        ow[w] = *reinterpret_cast<const f32x4*>(lds + w * WREG + row * OROW + x * 16);
      }
      float Mx = ml[0][0];
#pragma unroll
      for (int w = 1; w < NW; ++w) Mx = fmaxf(Mx, ml[w][0]);
      const float ms = (Mx == -INFINITY) ? 0.f : Mx * C2;
      float L = 0.f;
      f32x4 o = {0.f, 0.f, 0.f, 0.f};
#pragma unroll
      for (int w = 0; w < NW; ++w) {
        const float f = __builtin_amdgcn_exp2f(fmaf(ml[w][0], C2, -ms));
        L = fmaf(f, ml[w][1], L);
        o += f * ow[w];
      }
      const unsigned slot = (unsigned)(kvh * 32 + row) * WGH + wi;
      const unsigned e0 = (__float_as_uint(o[0]) & ~1u) | ((__float_as_uint(Mx) >> x) & 1u);
      const unsigned e1 = (__float_as_uint(o[1]) & ~1u) | ((__float_as_uint(L) >> x) & 1u);
      __builtin_amdgcn_raw_buffer_store_b128(u32x4{canon(e0), canon(e1), canon(__float_as_uint(o[2])), canon(__float_as_uint(o[3]))}, wrs,
                                             W_O + slot * (HD * 4) + x * 16, 0, 16);
    }
  }
  STAMP(10);
  if (wave == NW - 1) {
    if (wi == 0) __builtin_amdgcn_raw_buffer_store_b128(tag4, wrs, lane < 8 ? lane * 16 : OOB, W_EPOCH + kvh * 128, 16);
    constexpr int NL = 6;
    const int hw = lane >> 5, x = lane & 31;
    float Mx = -INFINITY, L = 0.f;
    f32x4 o = {0.f, 0.f, 0.f, 0.f};
    unsigned m = pmask;
    while (m) {
      unsigned vo[NL];
#pragma unroll
      for (int k = 0; k < NL; ++k) {
        const unsigned s0 = m ? (rowslot + __builtin_ctz(m)) * (HD * 4) + W_O : OOB;
        m &= m - 1;
        const unsigned s1 = m ? (rowslot + __builtin_ctz(m)) * (HD * 4) + W_O : OOB;
        m &= m - 1;
        vo[k] = (hw ? s1 : s0) + x * 16;
      }
      u32x4 ov[NL];
#pragma nounroll
      for (unsigned it = 0; it < SPIN; ++it) {
#pragma unroll
        for (int k = 0; k < NL; ++k) ov[k] = __builtin_amdgcn_raw_buffer_load_b128(wrs, vo[k], 0, 16);
        bool ok = true;
#pragma unroll
        for (int k = 0; k < NL; ++k) ok &= (ov[k][0] != SENT) & (ov[k][1] != SENT) & (ov[k][2] != SENT) & (ov[k][3] != SENT);
        if (__all(ok)) break;
        asm volatile("s_sleep 1" ::: "memory");
      }
      float mk[NL], lk[NL], mn = Mx;
#pragma unroll
      for (int k = 0; k < NL; ++k) {
        const u64 bm = __ballot(ov[k][0] & 1), bl = __ballot(ov[k][1] & 1);
        mk[k] = vo[k] >= OOB ? -INFINITY : __uint_as_float(hw ? (unsigned)(bm >> 32) : (unsigned)bm);
        lk[k] = __uint_as_float(hw ? (unsigned)(bl >> 32) : (unsigned)bl);
        mn = fmaxf(mn, mk[k]);
      }
      {
        auto s = __builtin_amdgcn_permlane32_swap(__float_as_uint(mn), __float_as_uint(mn), false, false);
        mn = fmaxf(__uint_as_float(s[0]), __uint_as_float(s[1]));
      }
      const float ms = (mn == -INFINITY) ? 0.f : mn * C2;
      const float alpha = __builtin_amdgcn_exp2f(Mx * C2 - ms);
      Mx = mn;
      L *= alpha;
      o *= alpha;
#pragma unroll
      for (int k = 0; k < NL; ++k) {
        const float w = __builtin_amdgcn_exp2f(fmaf(mk[k], C2, -ms));
        L = fmaf(w, lk[k], L);
        o += w * __builtin_bit_cast(f32x4, ov[k]);
      }
    }
    {
      auto s = __builtin_amdgcn_permlane32_swap(__float_as_uint(L), __float_as_uint(L), false, false);
      L = __uint_as_float(s[0]) + __uint_as_float(s[1]);
    }
#pragma unroll
    for (int r = 0; r < 4; ++r) {
      auto s = __builtin_amdgcn_permlane32_swap(__float_as_uint(o[r]), __float_as_uint(o[r]), false, false);
      o[r] = __uint_as_float(s[0]) + __uint_as_float(s[1]);
    }
    const int d0 = 32 * (x >> 3) + (x & 7);
    const int lb = clen;
    if (lb < LK && !(Mx * SCALE >= MASKV + 256.f)) {
      const float Mn = Mx * SCALE, Mt = fmaxf(Mn, MASKV);
      const float wv = (L > 0.f) ? __expf(Mn - Mt) : 0.f, wm = __expf(MASKV - Mt);
      f32x4 sv = {0.f, 0.f, 0.f, 0.f};
      for (int pos = lb; pos < LK; ++pos) {
        const int phys = *reinterpret_cast<const int*>(lds + L_BT + (cb * NPG + (pos >> 4)) * 4);
        const unsigned short* vp = vc + ((size_t)phys * PS + (pos & 15)) * (NKV * HD) + kvh * HD + d0;
#pragma unroll
        for (int r = 0; r < 4; ++r) sv[r] += __uint_as_float((unsigned)vp[8 * r] << 16);
      }
      o = o * wv + sv * wm;
      L = L * wv + wm * (float)(LK - lb);
    }
    const float inv = 1.f / L;
    unsigned short* op = out + (cb * NH + kvh * 4 + ch) * HD + d0 + 16 * hw;
    op[0] = __builtin_bit_cast(unsigned short, (__bf16)((hw ? o[2] : o[0]) * inv));
    op[8] = __builtin_bit_cast(unsigned short, (__bf16)((hw ? o[3] : o[1]) * inv));
  }
  STAMP_FLUSH(ws, blockIdx.x * NW + wave);
}

}

extern "C" void kernel_launch(void* const* d_in, const int* in_sizes, int n_in, void* d_out, int out_size, void* d_ws, size_t ws_size,
                              hipStream_t stream) {
  attn_kernel<<<NKV * WGH, NW * 64, 0, stream>>>((const unsigned short*)d_in[0], (const unsigned short*)d_in[1], (const unsigned short*)d_in[2],
                                                 (const int*)d_in[3], (const int*)d_in[4], (unsigned short*)d_out, (unsigned char*)d_ws,
                                                 (unsigned)in_sizes[1] * 2u);
}
```

```cpp
#define STAMPS
#define DIAG_I 7
#define DIAG_MIN 0
#define DIAG_HEAD 0
#include <hip/hip_runtime.h>
#include <hip/hip_bf16.h>
#include <stdint.h>
#include <type_traits>

typedef __attribute__((ext_vector_type(8))) __bf16 bf16x8;
typedef __attribute__((ext_vector_type(4))) float f32x4;
typedef __attribute__((ext_vector_type(2))) float f32x2;
typedef __attribute__((ext_vector_type(4))) unsigned u32x4;
typedef __attribute__((ext_vector_type(2))) unsigned u32x2;
typedef unsigned long long u64;

#include <cstdio>
#include <vector>
#include <algorithm>
#include <cstdlib>
constexpr int NST = 12;
constexpr size_t ST_OFF = (size_t)128 << 20;
#define STAMP(i) do { __builtin_amdgcn_sched_barrier(0); st_[i] = __builtin_amdgcn_s_memrealtime(); __builtin_amdgcn_sched_barrier(0); } while (0)
#define STAMP_DECL unsigned long long st_[NST] = {}
#define STAMP_FLUSH(ws, slot) do { if ((threadIdx.x & 63) == 0) { unsigned long long* p_ = (unsigned long long*)((char*)(ws) + ST_OFF) + (size_t)(slot) * NST; for (int i_ = 0; i_ < NST; ++i_) p_[i_] = st_[i_]; } } while (0)
static void dump_stamps(void* ws) {
  const int n = 2048;
  std::vector<unsigned long long> h((size_t)n * NST);
  (void)hipMemcpy(h.data(), (char*)ws + ST_OFF, h.size() * 8, hipMemcpyDeviceToHost);
  unsigned long long base = ~0ull;
  for (int w = 0; w < 2048; ++w) base = std::min(base, h[(size_t)w * NST]);
  for (int i = 0; i < NST; ++i) {
    std::vector<double> v;
    for (int w = 0; w < 2048; ++w) { unsigned long long t = h[(size_t)w * NST + i]; if (t >= base && t - base < 100000) v.push_back((t - base) * 0.01); }
    if (v.empty()) continue;
    std::sort(v.begin(), v.end());
    printf("  st%-2d n=%4zu min %6.2f p10 %6.2f med %6.2f p90 %6.2f max %6.2f\n", i, v.size(), v[0], v[v.size() / 10], v[v.size() / 2], v[v.size() * 9 / 10], v.back());
  }
  if (getenv("DETAIL")) {
    const int si = atoi(getenv("DETAIL"));
    printf("  per-WG max of st%d (rows: head, cols: wi); last col: head's first start\n", si);
    for (int k = 0; k < 8; ++k) {
      unsigned long long s0 = ~0ull;
      for (int wi = 0; wi < 32; ++wi) {
        unsigned long long m = 0;
        for (int w = 0; w < 8; ++w) { m = std::max(m, h[(size_t)((wi * 8 + k) * 8 + w) * NST + si]); s0 = std::min(s0, h[(size_t)((wi * 8 + k) * 8 + w) * NST]); }
        printf(" %4.1f", (m - base) * 0.01);
      }
      printf(" | %4.2f\n", (s0 - base) * 0.01);
    }
    printf("  per-wave (WG 80..83) stamps:\n");
    for (int b = 80; b < 84; ++b) for (int w = 0; w < 8; ++w) { printf("   b%d w%d:", b, w); for (int i = 0; i < NST; ++i) printf(" %5.2f", (h[(size_t)(b * 8 + w) * NST + i] - base) * 0.01); printf("\n"); }
  }
}

namespace {
constexpr int NB = 8, NH = 32, NKV = 8, HD = 128, NPG = 256, PS = 16, LK = NPG * PS;
constexpr int WGH = 32;
constexpr int NW = 8;
constexpr int NP = 4;
constexpr float SCALE = 0.08838834764831845f;
constexpr float C2 = SCALE * 1.4426950408889634f;
constexpr float MASKV = -998244352.0f;
constexpr unsigned TOKB = NKV * HD * 2, PAGEB = PS * TOKB;
constexpr unsigned OOB = 0x80000000u;
constexpr unsigned SENT = 0xFFFFFFFFu;
constexpr unsigned SPIN = 1u << 18;
constexpr unsigned CODE_PF = 14 * 1024;

constexpr int PIECE = 1040, KPAGE = 4 * PIECE, OROW = 528, WREG = 32 * OROW;
constexpr int L_Q = NW * WREG, L_BT = L_Q + NB * PIECE, L_ML = L_BT + NB * NPG * 4, L_FIRST = L_ML + NW * 32 * 8, L_CNT = L_FIRST + 32, L_END = L_CNT + 16;
static_assert(NP * KPAGE <= WREG && L_END <= 160 * 1024, "lds");

constexpr unsigned W_EPOCH = 0;
constexpr unsigned W_FLAG = NKV * 128;
constexpr unsigned W_O = W_FLAG + NKV * 32 * 128;
constexpr unsigned W_END = W_O + NKV * 32 * WGH * HD * 4;

__device__ __forceinline__ float xg_max(float x) {
  auto s = __builtin_amdgcn_permlane16_swap(__float_as_uint(x), __float_as_uint(x), false, false);
  x = fmaxf(__uint_as_float(s[0]), __uint_as_float(s[1]));
  auto t = __builtin_amdgcn_permlane32_swap(__float_as_uint(x), __float_as_uint(x), false, false);
  return fmaxf(__uint_as_float(t[0]), __uint_as_float(t[1]));
}
__device__ __forceinline__ float xg_sum(float x) {
  auto s = __builtin_amdgcn_permlane16_swap(__float_as_uint(x), __float_as_uint(x), false, false);
  x = __uint_as_float(s[0]) + __uint_as_float(s[1]);
  auto t = __builtin_amdgcn_permlane32_swap(__float_as_uint(x), __float_as_uint(x), false, false);
  return __uint_as_float(t[0]) + __uint_as_float(t[1]);
}
template <int CTRL> __device__ __forceinline__ float dpp(float x) {
  return __builtin_bit_cast(float, __builtin_amdgcn_mov_dpp(__builtin_bit_cast(int, x), CTRL, 0xf, 0xf, true));
}
template <int CTRL> __device__ __forceinline__ int dppi(int x) { return __builtin_amdgcn_mov_dpp(x, CTRL, 0xf, 0xf, true); }
__device__ __forceinline__ int min8(int x) { x = min(x, dppi<0xB1>(x)); x = min(x, dppi<0x4E>(x)); return min(x, dppi<0x141>(x)); }
__device__ __forceinline__ int max8(int x) { x = max(x, dppi<0xB1>(x)); x = max(x, dppi<0x4E>(x)); return max(x, dppi<0x141>(x)); }
__device__ __forceinline__ unsigned min8u(unsigned x) {
  x = min(x, (unsigned)dppi<0xB1>((int)x)); x = min(x, (unsigned)dppi<0x4E>((int)x)); return min(x, (unsigned)dppi<0x141>((int)x));
}
__device__ __forceinline__ float row_max(float x) {
  x = fmaxf(x, dpp<0xB1>(x)); x = fmaxf(x, dpp<0x4E>(x)); x = fmaxf(x, dpp<0x141>(x)); return fmaxf(x, dpp<0x128>(x));
}
__device__ __forceinline__ float row_sum(float x) {
  x += dpp<0xB1>(x); x += dpp<0x4E>(x); x += dpp<0x141>(x); return x + dpp<0x128>(x);
}
__device__ __forceinline__ unsigned pk_bf16(float lo, float hi) {
  typedef __attribute__((ext_vector_type(2))) __bf16 bf16x2;
  bf16x2 v = {(__bf16)lo, (__bf16)hi};
  return __builtin_bit_cast(unsigned, v);
}
__device__ __forceinline__ f32x4 mfma(u32x4 a, u32x4 b, f32x4 c) {
  return __builtin_amdgcn_mfma_f32_16x16x32_bf16(__builtin_bit_cast(bf16x8, a), __builtin_bit_cast(bf16x8, b), c, 0, 0, 0);
}
__device__ __forceinline__ unsigned canon(unsigned u) { return u == SENT ? 0x7FFFFFFFu : u; }
template <int N> __device__ __forceinline__ void wait_vm() { asm volatile("s_waitcnt vmcnt(%0)" ::"n"(N) : "memory"); }
template <int LO, int SO> __device__ __forceinline__ void dma_nt(unsigned lds, unsigned voff, __amdgpu_buffer_rsrc_t rs, unsigned soff) {
  unsigned t;
  asm volatile("s_add_u32 m0, %1, %5\n\ts_add_u32 %0, %4, %6\n\tbuffer_load_dwordx4 %2, %3, %0 offen nt lds"
               : "=&s"(t) : "s"(lds), "v"(voff), "s"(rs), "s"(soff), "n"(LO), "n"(SO) : "memory", "scc");
}
template <int P> __device__ __forceinline__ void dma_page(unsigned lds, unsigned voff, __amdgpu_buffer_rsrc_t rs, unsigned soff) {
  dma_nt<P * KPAGE, 0>(lds, voff, rs, soff);
  dma_nt<P * KPAGE + PIECE, 4 * TOKB>(lds, voff, rs, soff);
  dma_nt<P * KPAGE + 2 * PIECE, 8 * TOKB>(lds, voff, rs, soff);
  dma_nt<P * KPAGE + 3 * PIECE, 12 * TOKB>(lds, voff, rs, soff);
}
__device__ __forceinline__ void dma(unsigned lds, unsigned voff, __amdgpu_buffer_rsrc_t rs, unsigned soff) {
  asm volatile("s_mov_b32 m0, %0\n\ts_nop 0\n\tbuffer_load_dwordx4 %1, %2, %3 offen lds" ::"s"(lds), "v"(voff), "s"(rs), "s"(soff) : "memory");
}

__global__ __launch_bounds__(NW * 64, 2) void attn_kernel(const unsigned short* __restrict__ q, const unsigned short* __restrict__ kc,
                                                          const unsigned short* __restrict__ vc, const int* __restrict__ bt,
                                                          const int* __restrict__ lens, unsigned short* __restrict__ out,
                                                          unsigned char* __restrict__ ws, unsigned kv_bytes) {
  __shared__ __attribute__((aligned(16))) unsigned char lds[L_END];
  STAMP_DECL;
  STAMP(0);
  unsigned pf_dummy;
  {
    u64 pc;
    asm volatile("s_getpc_b64 %1\n\tglobal_load_dword %0, %2, %1" : "=&v"(pf_dummy), "=&s"(pc) : "v"(min(threadIdx.x * 128u, CODE_PF - 128u)));
  }
  const int tid = threadIdx.x, lane = tid & 63, wave = __builtin_amdgcn_readfirstlane(tid >> 6);
  const int g = lane >> 4, c = lane & 15;
  const int kvh = blockIdx.x & (NKV - 1), wi = blockIdx.x >> 3;
  const unsigned ldsb = (unsigned)(size_t)lds;
  asm volatile("" ::"s"(q), "s"(kc), "s"(vc), "s"(bt), "s"(lens), "s"(out), "s"(ws), "s"(kv_bytes));

  const int sb = lane & 7;
  const u32x4 tv = *reinterpret_cast<const u32x4*>(bt + wave * NPG + 4 * lane);
  const u32x4 t0 = *reinterpret_cast<const u32x4*>(bt + 4 * lane);
  int len = lens[sb];
  const u64 tag = *(const __attribute__((address_space(4))) u64*)(ws + W_EPOCH + kvh * 128) + 1;
  __builtin_amdgcn_sched_barrier(0);
  const unsigned swz = (unsigned)(c ^ ((g & 2) << 2)) * 16;
  {
    const __amdgpu_buffer_rsrc_t qrs = __builtin_amdgcn_make_buffer_rsrc((void*)q, 0, NB * NH * HD * 2, 0x00020000);
    dma(ldsb + L_Q + wave * PIECE, g * 256 + swz, qrs, (wave * NH + kvh * 4) * HD * 2);
  }
  const __amdgpu_buffer_rsrc_t krs = __builtin_amdgcn_make_buffer_rsrc((void*)(kc + kvh * HD), 0, kv_bytes - kvh * HD * 2, 0x00020000);
  const __amdgpu_buffer_rsrc_t vrs = __builtin_amdgcn_make_buffer_rsrc((void*)(vc + kvh * HD), 0, kv_bytes - kvh * HD * 2, 0x00020000);
  const __amdgpu_buffer_rsrc_t wrs = __builtin_amdgcn_make_buffer_rsrc((void*)ws, 0, W_END, 0x00020000);
  const unsigned rowslot = (unsigned)(kvh * 32 + wi) * WGH;
  if (tid == 0) *reinterpret_cast<int*>(lds + L_CNT) = 0;
  unsigned voff = g * TOKB + c * 16, koff = g * TOKB + swz;
  const unsigned wreg = wave * WREG;
  unsigned krd = wreg + (c & 3) * PIECE + (c >> 2) * 256 + ((4 * g) ^ (c & 8)) * 16;
  unsigned qrd = L_Q + (c & 3) * PIECE + (c >> 2) * 256 + ((4 * g) ^ (c & 8)) * 16;
  f32x4 acc[2][8];
#pragma unroll
  for (int t = 0; t < 2; ++t)
#pragma unroll
    for (int d = 0; d < 8; ++d) acc[t][d] = f32x4{0.f, 0.f, 0.f, 0.f};
  float M[2] = {-INFINITY, -INFINITY}, l[2] = {0.f, 0.f};
  unsigned kdst = ldsb + wreg;
  asm volatile("" : "+v"(voff), "+v"(koff), "+v"(krd), "+v"(qrd), "+s"(kdst) : "s"(krs), "s"(vrs), "s"(wrs));
  __builtin_amdgcn_sched_barrier(0);
  len = min(max(len, 0), LK);
  const int np = (len + 15) >> 4;
  int psn = np;
  { const int t = dppi<0x111>(psn); psn += sb >= 1 ? t : 0; }
  { const int t = dppi<0x112>(psn); psn += sb >= 2 ? t : 0; }
  { const int t = dppi<0x114>(psn); psn += sb >= 4 ? t : 0; }
  int ps8 = __builtin_amdgcn_readlane(psn, 7);
  {
    int first = min8(len >> 4);
#pragma unroll
    for (int k = 0; k < 4; ++k) {
      const u64 m = __ballot(tv[k] != t0[k]);
      first = min(first, m ? 4 * (int)__builtin_ctzll(m) + k : NPG);
    }
    *reinterpret_cast<u32x4*>(lds + L_BT + wave * (NPG * 4) + lane * 16) = tv;
    if (lane == 0) *reinterpret_cast<int*>(lds + L_FIRST + wave * 4) = first;
  }
  int uo = wave + (lane & 56), bta = L_BT + sb * (NPG * 4);
  asm volatile("" : "+v"(psn), "+s"(ps8), "+v"(uo), "+v"(bta));
  STAMP(1);
  asm volatile("s_waitcnt vmcnt(0) lgkmcnt(0)\n\ts_barrier" ::: "memory");
  asm volatile("" ::"v"(pf_dummy));
  STAMP(2);
  int Sv, bnd;
  {
    const u32x4 a = *reinterpret_cast<const u32x4*>(lds + L_FIRST), b = *reinterpret_cast<const u32x4*>(lds + L_FIRST + 16);
    Sv = (int)min(min(min(a[0], a[1]), min(a[2], a[3])), min(min(b[0], b[1]), min(b[2], b[3])));
    Sv = Sv < 8 ? 0 : Sv;
    asm volatile("" : "+v"(Sv));
    const int P = ps8 - NB * Sv;
    const float fS = (float)Sv, fP = (float)P;
    const float fn = 288.f * fS * __builtin_amdgcn_rcpf(fmaf(9.f, fS, fmaf(8.f, fP, 1e-3f)));
    const int nsh = min(max((int)(fn + 0.5f), (Sv + 63) >> 6), WGH - ((P + 63) >> 6));
    const bool lo = lane <= nsh;
    const int cnt = lo ? nsh : WGH - nsh, tot = lo ? Sv : P, i = lo ? lane : lane - nsh;
    const int qq = (int)(((lo ? fS : fP) + 0.5f) * __builtin_amdgcn_rcpf((float)max(cnt, 1)));
    bnd = (lo ? 0 : Sv) + i * qq + min(i, tot - qq * cnt);
  }
  const int u0 = __builtin_amdgcn_readlane(bnd, wi), u1 = __builtin_amdgcn_readlane(bnd, wi + 1);
  const int S = __builtin_amdgcn_readfirstlane(Sv);
  const int offn = psn - sb * Sv, offb = offn - np + Sv;
  unsigned so_v;
  int info_v;
  {
    const int u = u0 + uo;
    const bool sh = u < S;
    const bool hit = (u < u1) & (sh ? sb == 0 : (u >= offb) & (u < offn));
    const int j = sh ? u : min(max(S + u - offb, 0), NPG - 1);
    int phys = *reinterpret_cast<const int*>(lds + bta + j * 4);
    asm volatile("" : "+v"(phys));
    so_v = (unsigned)min8u(hit ? (unsigned)phys * PAGEB : OOB);
    info_v = hit ? (sh ? 16 | 8 << 8 : min(16, len - 16 * j) | sb << 8) : 0;
  }
  const int cb = (wi >> 4) * 4 + (wi & 3), ch = (wi & 15) >> 2;

  u32x2 fl;
  unsigned flo;
  const u32x4 tag4 = {(unsigned)tag, (unsigned)(tag >> 32), (unsigned)tag, (unsigned)(tag >> 32)};
  int blo, nseq, clen;
  unsigned pmask;

  auto round = [&](auto rdc) __attribute__((always_inline)) {
    constexpr int rd = decltype(rdc)::value;
    unsigned so[NP];
#pragma unroll
    for (int p = 0; p < NP; ++p) so[p] = __builtin_amdgcn_readlane(so_v, 8 * (NP * rd + p));
    STAMP(3);
    __builtin_amdgcn_s_setprio(1);
    dma_page<0>(kdst, koff, krs, so[0]);
    __builtin_amdgcn_sched_barrier(0);
    if constexpr (rd == 0) {
      const int cofl = __builtin_amdgcn_readlane(offb, cb), cofh = __builtin_amdgcn_readlane(offn, cb);
      const int a0 = bnd, a1 = __builtin_amdgcn_update_dpp(0, bnd, 0x130, 0xf, 0xf, false);
      pmask = (unsigned)__ballot((a1 > a0) & ((a0 < S) | ((a0 < cofh) & (a1 > cofl))));
      const u32x4 sv = {SENT, SENT, SENT, SENT};
      const unsigned pm = pmask >> (4 * wave + (lane >> 5));
      __builtin_amdgcn_raw_buffer_store_b128(sv, wrs, (pm & 1) ? lane * 16 : OOB, W_O + (rowslot + 4 * wave) * (HD * 4), 16);
      __builtin_amdgcn_raw_buffer_store_b128(sv, wrs, (pm & 4) ? lane * 16 + 1024 : OOB, W_O + (rowslot + 4 * wave) * (HD * 4), 16);
    }
    __builtin_amdgcn_sched_barrier(0);
    dma_page<1>(kdst, koff, krs, so[1]);
    dma_page<2>(kdst, koff, krs, so[2]);
    dma_page<3>(kdst, koff, krs, so[3]);
    __builtin_amdgcn_s_setprio(0);
    __builtin_amdgcn_sched_barrier(0);
    STAMP(4);
    int info[NP];
    {
      const int inf = max8(info_v);
#pragma unroll
      for (int p = 0; p < NP; ++p) info[p] = __builtin_amdgcn_readlane(inf, 8 * (NP * rd + p));
    }
    if constexpr (rd == 0) {
      const unsigned sm = (unsigned)__ballot((u1 > u0) & ((u0 < S) | ((u0 < offn) & (u1 > offb)))) & 255u;
      blo = sm ? __builtin_ctz(sm) : 0;
      nseq = __builtin_popcount(sm);
      clen = __builtin_amdgcn_readlane(len, cb);
      asm volatile("" : "+s"(blo), "+s"(nseq), "+s"(clen), "+s"(pmask));
      const bool need = (wave == NW - 1) & (lane < 32) & ((wi == 0) | ((unsigned)((lane >> 4) * 4 + (lane & 3) - blo) < (unsigned)nseq));
      flo = need ? W_FLAG + (kvh * 32 + lane) * 128 : OOB;
    }
    u32x4 qf[2][4];
#pragma unroll
    for (int t = 0; t < 2; ++t)
#pragma unroll
      for (int s = 0; s < 4; ++s) qf[t][s] = *reinterpret_cast<const u32x4*>(lds + qrd + t * 4 * PIECE + s * 16);

    u32x4 vf[NP][4];
    f32x4 sc[NP][2];
    float mx[2] = {M[0], M[1]};
#pragma unroll
    for (int p = 0; p < NP; ++p) {
      if (p == 0) wait_vm<rd == 0 ? 14 : 12>();
      if (p == 1) wait_vm<8>();
      if (p == 2) wait_vm<4>();
      if (p == 3) wait_vm<0>();
      if (p == 1 && rd == 0) {
        int old = 0;
        if (lane == 0) old = __hip_atomic_fetch_add(reinterpret_cast<int*>(lds + L_CNT), 1, __ATOMIC_RELAXED, __HIP_MEMORY_SCOPE_WORKGROUP);
        if (__builtin_amdgcn_readfirstlane(old) == NW - 1)
          __builtin_amdgcn_raw_buffer_store_b128(tag4, wrs, lane < 8 ? lane * 16 : OOB, W_FLAG + (kvh * 32 + wi) * 128, 16);
      }
      u32x4 kf[4];
#pragma unroll
      for (int s = 0; s < 4; ++s) kf[s] = *reinterpret_cast<const u32x4*>(lds + krd + p * KPAGE + s * 16);
      const int nv = info[p] & 255, sq = info[p] >> 8;
#pragma unroll
      for (int t = 0; t < 2; ++t) {
        f32x4 a = {0.f, 0.f, 0.f, 0.f};
#pragma unroll
        for (int s = 0; s < 4; ++s) a = mfma(kf[s], qf[t][s], a);
        const bool cv = (sq == 8) | (sq == 4 * t + (c & 3));
#pragma unroll
        for (int r = 0; r < 4; ++r) {
          a[r] = (cv & (4 * r + g < nv)) ? a[r] : -INFINITY;
          mx[t] = fmaxf(mx[t], a[r]);
        }
        sc[p][t] = a;
      }
    }
    __builtin_amdgcn_sched_barrier(0);
#pragma unroll
    for (int p = 0; p < NP; ++p)
#pragma unroll
      for (int i = 0; i < 4; ++i) vf[p][i] = __builtin_amdgcn_raw_buffer_load_b128(vrs, voff, so[p] + i * 4 * TOKB, 2);
    fl = __builtin_amdgcn_raw_buffer_load_b64(wrs, flo, 0, 16);
    STAMP(5);
    __builtin_amdgcn_sched_barrier(0);
    u32x4 pf[NP / 2][2];
#pragma unroll
    for (int t = 0; t < 2; ++t) {
      const float mn = xg_max(mx[t]);
      const float ms = (mn == -INFINITY) ? 0.f : mn * C2;
      float ls = 0.f;
      if constexpr (rd != 0) {
        const float alpha = __builtin_amdgcn_exp2f(M[t] * C2 - ms);
        ls = l[t] * alpha;
#pragma unroll
        for (int d = 0; d < 8; ++d) acc[t][d] *= alpha;
      }
      M[t] = mn;
#pragma unroll
      for (int p = 0; p < NP; ++p) {
        float e[4];
#pragma unroll
        for (int r = 0; r < 4; ++r) { e[r] = __builtin_amdgcn_exp2f(fmaf(sc[p][t][r], C2, -ms)); ls += e[r]; }
        pf[p >> 1][t][(p & 1) * 2] = pk_bf16(e[0], e[1]);
        pf[p >> 1][t][(p & 1) * 2 + 1] = pk_bf16(e[2], e[3]);
      }
      l[t] = ls;
    }
    STAMP(6);
    __builtin_amdgcn_sched_barrier(0);
#pragma unroll
    for (int pp = 0; pp < NP / 2; ++pp) {
#pragma unroll
      for (int e = 0; e < 4; ++e) {
        u32x4 lo, hi;
#pragma unroll
        for (int h = 0; h < 2; ++h) {
          const u32x4* v = vf[2 * pp + h];
          lo[2 * h] = __builtin_amdgcn_perm(v[1][e], v[0][e], 0x05040100u);
          lo[2 * h + 1] = __builtin_amdgcn_perm(v[3][e], v[2][e], 0x05040100u);
          hi[2 * h] = __builtin_amdgcn_perm(v[1][e], v[0][e], 0x07060302u);
          hi[2 * h + 1] = __builtin_amdgcn_perm(v[3][e], v[2][e], 0x07060302u);
        }
#pragma unroll
        for (int t = 0; t < 2; ++t) {
          acc[t][2 * e] = mfma(lo, pf[pp][t], acc[t][2 * e]);
          acc[t][2 * e + 1] = mfma(hi, pf[pp][t], acc[t][2 * e + 1]);
        }
      }
      __builtin_amdgcn_sched_barrier(0);
    }
  };
  round(std::integral_constant<int, 0>{});
  if (u1 - u0 > NW * NP) round(std::integral_constant<int, 1>{});
  STAMP(7);
#pragma unroll
  for (int t = 0; t < 2; ++t) {
    const float ls = xg_sum(l[t]);
    if (g == 0) *reinterpret_cast<f32x2*>(lds + L_ML + (wave * 32 + 16 * t + c) * 8) = f32x2{M[t], ls};
    if ((unsigned)(4 * t + (c & 3) - blo) < (unsigned)nseq) {
#pragma unroll
      for (int d = 0; d < 8; ++d) *reinterpret_cast<f32x4*>(lds + wreg + (16 * t + c) * OROW + g * 128 + d * 16) = acc[t][d];
    }
  }
#pragma nounroll
  for (unsigned it = 0; it < SPIN; ++it) {
    const bool ok = (flo == OOB) | ((fl[0] == (unsigned)tag) & (fl[1] == (unsigned)(tag >> 32)));
    if (__all(ok)) break;
    asm volatile("s_sleep 2" ::: "memory");
    fl = __builtin_amdgcn_raw_buffer_load_b64(wrs, flo, 0, 16);
  }
  STAMP(8);
  __syncthreads();
  STAMP(9);
  {
    const int x = tid & 31;
    for (int k = tid >> 5; k < 4 * nseq; k += NW * 2) {
      const int b = blo + (k >> 2), row = (b >> 2) * 16 + (k & 3) * 4 + (b & 3);
      f32x2 ml[NW];
      f32x4 ow[NW];
#pragma unroll
      for (int w = 0; w < NW; ++w) {
        ml[w] = *reinterpret_cast<const f32x2*>(lds + L_ML + (w * 32 + row) * 8);
        ow[w] = *reinterpret_cast<const f32x4*>(lds + w * WREG + row * OROW + x * 16);
      }
      float Mx = ml[0][0];
#pragma unroll
      for (int w = 1; w < NW; ++w) Mx = fmaxf(Mx, ml[w][0]);
      const float ms = (Mx == -INFINITY) ? 0.f : Mx * C2;
      float L = 0.f;
      f32x4 o = {0.f, 0.f, 0.f, 0.f};
#pragma unroll
      for (int w = 0; w < NW; ++w) {
        const float f = __builtin_amdgcn_exp2f(fmaf(ml[w][0], C2, -ms));
        L = fmaf(f, ml[w][1], L);
        o += f * ow[w];
      }
      const unsigned slot = (unsigned)(kvh * 32 + row) * WGH + wi;
      const unsigned e0 = (__float_as_uint(o[0]) & ~1u) | ((__float_as_uint(Mx) >> x) & 1u);
      const unsigned e1 = (__float_as_uint(o[1]) & ~1u) | ((__float_as_uint(L) >> x) & 1u);
      __builtin_amdgcn_raw_buffer_store_b128(u32x4{canon(e0), canon(e1), canon(__float_as_uint(o[2])), canon(__float_as_uint(o[3]))}, wrs,
                                             W_O + slot * (HD * 4) + x * 16, 0, 16);
    }
  }
  STAMP(10);
  if (wave == NW - 1) {
    if (wi == 0) __builtin_amdgcn_raw_buffer_store_b128(tag4, wrs, lane < 8 ? lane * 16 : OOB, W_EPOCH + kvh * 128, 16);
    constexpr int NL = 6;
    const int hw = lane >> 5, x = lane & 31;
    float Mx = -INFINITY, L = 0.f;
    f32x4 o = {0.f, 0.f, 0.f, 0.f};
    unsigned m = pmask;
    while (m) {
      unsigned vo[NL];
#pragma unroll
      for (int k = 0; k < NL; ++k) {
        const unsigned s0 = m ? (rowslot + __builtin_ctz(m)) * (HD * 4) + W_O : OOB;
        m &= m - 1;
        const unsigned s1 = m ? (rowslot + __builtin_ctz(m)) * (HD * 4) + W_O : OOB;
        m &= m - 1;
        vo[k] = (hw ? s1 : s0) + x * 16;
      }
      u32x4 ov[NL];
      unsigned po[NL];
#pragma unroll
      for (int k = 0; k < NL; ++k) { po[k] = vo[k]; ov[k] = u32x4{0u, 0u, 0u, 0u}; }
#pragma nounroll
      for (unsigned it = 0; it < SPIN; ++it) {
        u32x4 nv[NL];
#pragma unroll
        for (int k = 0; k < NL; ++k) nv[k] = __builtin_amdgcn_raw_buffer_load_b128(wrs, po[k], 0, 16);
        bool ok = true;
#pragma unroll
        for (int k = 0; k < NL; ++k) {
          const bool got = (nv[k][0] != SENT) & (nv[k][1] != SENT) & (nv[k][2] != SENT) & (nv[k][3] != SENT);
          ov[k] = po[k] >= OOB ? ov[k] : nv[k];
          po[k] = got ? OOB : po[k];
          ok &= got;
        }
        if (__all(ok)) break;
        asm volatile("s_sleep 1" ::: "memory");
      }
      float mk[NL], lk[NL], mn = Mx;
#pragma unroll
      for (int k = 0; k < NL; ++k) {
        const u64 bm = __ballot(ov[k][0] & 1), bl = __ballot(ov[k][1] & 1);
        mk[k] = vo[k] >= OOB ? -INFINITY : __uint_as_float(hw ? (unsigned)(bm >> 32) : (unsigned)bm);
        lk[k] = __uint_as_float(hw ? (unsigned)(bl >> 32) : (unsigned)bl);
        mn = fmaxf(mn, mk[k]);
      }
      {
        auto s = __builtin_amdgcn_permlane32_swap(__float_as_uint(mn), __float_as_uint(mn), false, false);
        mn = fmaxf(__uint_as_float(s[0]), __uint_as_float(s[1]));
      }
      const float ms = (mn == -INFINITY) ? 0.f : mn * C2;
      const float alpha = __builtin_amdgcn_exp2f(Mx * C2 - ms);
      Mx = mn;
      L *= alpha;
      o *= alpha;
#pragma unroll
      for (int k = 0; k < NL; ++k) {
        const float w = __builtin_amdgcn_exp2f(fmaf(mk[k], C2, -ms));
        L = fmaf(w, lk[k], L);
        o += w * __builtin_bit_cast(f32x4, ov[k]);
      }
    }
    {
      auto s = __builtin_amdgcn_permlane32_swap(__float_as_uint(L), __float_as_uint(L), false, false);
      L = __uint_as_float(s[0]) + __uint_as_float(s[1]);
    }
#pragma unroll
    for (int r = 0; r < 4; ++r) {
      auto s = __builtin_amdgcn_permlane32_swap(__float_as_uint(o[r]), __float_as_uint(o[r]), false, false);
      o[r] = __uint_as_float(s[0]) + __uint_as_float(s[1]);
    }
    const int d0 = 32 * (x >> 3) + (x & 7);
    const int lb = clen;
    if (lb < LK && !(Mx * SCALE >= MASKV + 256.f)) {
      const float Mn = Mx * SCALE, Mt = fmaxf(Mn, MASKV);
      const float wv = (L > 0.f) ? __expf(Mn - Mt) : 0.f, wm = __expf(MASKV - Mt);
      f32x4 sv = {0.f, 0.f, 0.f, 0.f};
      for (int pos = lb; pos < LK; ++pos) {
        const int phys = *reinterpret_cast<const int*>(lds + L_BT + (cb * NPG + (pos >> 4)) * 4);
        const unsigned short* vp = vc + ((size_t)phys * PS + (pos & 15)) * (NKV * HD) + kvh * HD + d0;
#pragma unroll
        for (int r = 0; r < 4; ++r) sv[r] += __uint_as_float((unsigned)vp[8 * r] << 16);
      }
      o = o * wv + sv * wm;
      L = L * wv + wm * (float)(LK - lb);
    }
    const float inv = 1.f / L;
    unsigned short* op = out + (cb * NH + kvh * 4 + ch) * HD + d0 + 16 * hw;
    op[0] = __builtin_bit_cast(unsigned short, (__bf16)((hw ? o[2] : o[0]) * inv));
    op[8] = __builtin_bit_cast(unsigned short, (__bf16)((hw ? o[3] : o[1]) * inv));
  }
  asm volatile("s_waitcnt vmcnt(0)" ::: "memory");
  STAMP(11);
  STAMP_FLUSH(ws, blockIdx.x * NW + wave);
}

__global__ __launch_bounds__(256) void diag_k(const float* ws) {
  __shared__ unsigned long long sm[2][256];
  const unsigned long long* p = (const unsigned long long*)((const char*)ws + ST_OFF);
  unsigned long long mn = ~0ull, mx = DIAG_MIN ? ~0ull : 0;
  for (int w = threadIdx.x; w < 2048; w += 256) mn = min(mn, p[(size_t)w * NST]);
  for (int w = threadIdx.x; w < 2048; w += 256) {
    if (((w >> 3) & 7) != DIAG_HEAD) continue;
    unsigned long long t = p[(size_t)w * NST + DIAG_I]; mx = DIAG_MIN ? min(mx, t) : max(mx, t); }
  sm[0][threadIdx.x] = mn; sm[1][threadIdx.x] = mx;
  __syncthreads();
  if (threadIdx.x == 0) {
    for (int i = 0; i < 256; ++i) { mn = min(mn, sm[0][i]); mx = DIAG_MIN ? min(mx, sm[1][i]) : max(mx, sm[1][i]); }
    unsigned long long q = (mx - mn + 5) / 10;
    if (q > 400) q = 400;
    const unsigned long long target = mn + 10000 + 800 * q;
    for (int it = 0; it < 1000000; ++it) { if (__builtin_amdgcn_s_memrealtime() >= target) break; __builtin_amdgcn_s_sleep(1); }
  }
}
}

extern "C" void kernel_launch(void* const* d_in, const int* in_sizes, int n_in, void* d_out, int out_size, void* d_ws, size_t ws_size,
                              hipStream_t stream) {
  attn_kernel<<<NKV * WGH, NW * 64, 0, stream>>>((const unsigned short*)d_in[0], (const unsigned short*)d_in[1], (const unsigned short*)d_in[2],
                                                 (const int*)d_in[3], (const int*)d_in[4], (unsigned short*)d_out, (unsigned char*)d_ws,
                                                 (unsigned)in_sizes[1] * 2u);
  diag_k<<<1, 256, 0, stream>>>((const float*)d_ws);
}
```

```cpp
#include <hip/hip_runtime.h>
#include <hip/hip_bf16.h>
#include <stdint.h>
#include <type_traits>

typedef __attribute__((ext_vector_type(8))) __bf16 bf16x8;
typedef __attribute__((ext_vector_type(4))) float f32x4;
typedef __attribute__((ext_vector_type(2))) float f32x2;
typedef __attribute__((ext_vector_type(4))) unsigned u32x4;
typedef __attribute__((ext_vector_type(2))) unsigned u32x2;
typedef unsigned long long u64;

#define STAMP(i)
#define STAMP_DECL
#define STAMP_FLUSH(ws, slot)

namespace {
constexpr int NB = 8, NH = 32, NKV = 8, HD = 128, NPG = 256, PS = 16, LK = NPG * PS;
constexpr int WGH = 32;
constexpr int NW = 8;
constexpr int NP = 4;
constexpr float SCALE = 0.08838834764831845f;
constexpr float C2 = SCALE * 1.4426950408889634f;
constexpr float MASKV = -998244352.0f;
constexpr unsigned TOKB = NKV * HD * 2, PAGEB = PS * TOKB;
constexpr unsigned OOB = 0x80000000u;
constexpr unsigned SENT = 0xFFFFFFFFu;
constexpr unsigned SPIN = 1u << 18;
constexpr unsigned CODE_PF = 14 * 1024;

constexpr int PIECE = 1040, KPAGE = 4 * PIECE, OROW = 528, WREG = 32 * OROW;
constexpr int L_Q = NW * WREG, L_BT = L_Q + NB * PIECE, L_M = L_BT + NB * NPG * 4, L_L = L_M + 32 * NW * 4, L_FIRST = L_L + 32 * NW * 4, L_CNT = L_FIRST + 32, L_END = L_CNT + 16;
static_assert(NP * KPAGE <= WREG && L_END <= 160 * 1024, "lds");

constexpr unsigned W_EPOCH = 0;
constexpr unsigned W_FLAG = NKV * 128;
constexpr unsigned W_O = W_FLAG + NKV * 32 * 128;
constexpr unsigned W_END = W_O + NKV * 32 * WGH * HD * 4;

__device__ __forceinline__ float xg_max(float x) {
  auto s = __builtin_amdgcn_permlane16_swap(__float_as_uint(x), __float_as_uint(x), false, false);
  x = fmaxf(__uint_as_float(s[0]), __uint_as_float(s[1]));
  auto t = __builtin_amdgcn_permlane32_swap(__float_as_uint(x), __float_as_uint(x), false, false);
  return fmaxf(__uint_as_float(t[0]), __uint_as_float(t[1]));
}
__device__ __forceinline__ float xg_sum(float x) {
  auto s = __builtin_amdgcn_permlane16_swap(__float_as_uint(x), __float_as_uint(x), false, false);
  x = __uint_as_float(s[0]) + __uint_as_float(s[1]);
  auto t = __builtin_amdgcn_permlane32_swap(__float_as_uint(x), __float_as_uint(x), false, false);
  return __uint_as_float(t[0]) + __uint_as_float(t[1]);
}
template <int CTRL> __device__ __forceinline__ float dpp(float x) {
  return __builtin_bit_cast(float, __builtin_amdgcn_mov_dpp(__builtin_bit_cast(int, x), CTRL, 0xf, 0xf, true));
}
template <int CTRL> __device__ __forceinline__ int dppi(int x) { return __builtin_amdgcn_mov_dpp(x, CTRL, 0xf, 0xf, true); }
__device__ __forceinline__ int min8(int x) { x = min(x, dppi<0xB1>(x)); x = min(x, dppi<0x4E>(x)); return min(x, dppi<0x141>(x)); }
__device__ __forceinline__ int max8(int x) { x = max(x, dppi<0xB1>(x)); x = max(x, dppi<0x4E>(x)); return max(x, dppi<0x141>(x)); }
__device__ __forceinline__ unsigned min8u(unsigned x) {
  x = min(x, (unsigned)dppi<0xB1>((int)x)); x = min(x, (unsigned)dppi<0x4E>((int)x)); return min(x, (unsigned)dppi<0x141>((int)x));
}
__device__ __forceinline__ float row_max(float x) {
  x = fmaxf(x, dpp<0xB1>(x)); x = fmaxf(x, dpp<0x4E>(x)); x = fmaxf(x, dpp<0x141>(x)); return fmaxf(x, dpp<0x128>(x));
}
__device__ __forceinline__ float row_sum(float x) {
  x += dpp<0xB1>(x); x += dpp<0x4E>(x); x += dpp<0x141>(x); return x + dpp<0x128>(x);
}
__device__ __forceinline__ unsigned pk_bf16(float lo, float hi) {
  typedef __attribute__((ext_vector_type(2))) __bf16 bf16x2;
  bf16x2 v = {(__bf16)lo, (__bf16)hi};
  return __builtin_bit_cast(unsigned, v);
}
__device__ __forceinline__ f32x4 mfma(u32x4 a, u32x4 b, f32x4 c) {
  return __builtin_amdgcn_mfma_f32_16x16x32_bf16(__builtin_bit_cast(bf16x8, a), __builtin_bit_cast(bf16x8, b), c, 0, 0, 0);
}
__device__ __forceinline__ unsigned canon(unsigned u) { return u == SENT ? 0x7FFFFFFFu : u; }
template <int N> __device__ __forceinline__ void wait_vm() { asm volatile("s_waitcnt vmcnt(%0)" ::"n"(N) : "memory"); }
template <int LO, int SO> __device__ __forceinline__ void dma_nt(unsigned lds, unsigned voff, __amdgpu_buffer_rsrc_t rs, unsigned soff) {
  unsigned t;
  asm volatile("s_add_u32 m0, %1, %5\n\ts_add_u32 %0, %4, %6\n\tbuffer_load_dwordx4 %2, %3, %0 offen nt lds"
               : "=&s"(t) : "s"(lds), "v"(voff), "s"(rs), "s"(soff), "n"(LO), "n"(SO) : "memory", "scc");
}
template <int P> __device__ __forceinline__ void dma_page(unsigned lds, unsigned voff, __amdgpu_buffer_rsrc_t rs, unsigned soff) {
  dma_nt<P * KPAGE, 0>(lds, voff, rs, soff);
  dma_nt<P * KPAGE + PIECE, 4 * TOKB>(lds, voff, rs, soff);
  dma_nt<P * KPAGE + 2 * PIECE, 8 * TOKB>(lds, voff, rs, soff);
  dma_nt<P * KPAGE + 3 * PIECE, 12 * TOKB>(lds, voff, rs, soff);
}
__device__ __forceinline__ void dma(unsigned lds, unsigned voff, __amdgpu_buffer_rsrc_t rs, unsigned soff) {
  asm volatile("s_mov_b32 m0, %0\n\ts_nop 0\n\tbuffer_load_dwordx4 %1, %2, %3 offen lds" ::"s"(lds), "v"(voff), "s"(rs), "s"(soff) : "memory");
}

__global__ __launch_bounds__(NW * 64, 2) void attn_kernel(const unsigned short* __restrict__ q, const unsigned short* __restrict__ kc,
                                                          const unsigned short* __restrict__ vc, const int* __restrict__ bt,
                                                          const int* __restrict__ lens, unsigned short* __restrict__ out,
                                                          unsigned char* __restrict__ ws, unsigned kv_bytes) {
  __shared__ __attribute__((aligned(16))) unsigned char lds[L_END];
  STAMP_DECL;
  STAMP(0);
  unsigned pf_dummy;
  {
    u64 pc;
    asm volatile("s_getpc_b64 %1\n\tglobal_load_dword %0, %2, %1" : "=&v"(pf_dummy), "=&s"(pc) : "v"(min(threadIdx.x * 128u, CODE_PF - 128u)));
  }
  const int tid = threadIdx.x, lane = tid & 63, wave = __builtin_amdgcn_readfirstlane(tid >> 6);
  const int g = lane >> 4, c = lane & 15;
  const int kvh = blockIdx.x & (NKV - 1), wi = blockIdx.x >> 3;
  const unsigned ldsb = (unsigned)(size_t)lds;
  asm volatile("" ::"s"(q), "s"(kc), "s"(vc), "s"(bt), "s"(lens), "s"(out), "s"(ws), "s"(kv_bytes));

  const int sb = lane & 7;
  const u32x4 tv = *reinterpret_cast<const u32x4*>(bt + wave * NPG + 4 * lane);
  const u32x4 t0 = *reinterpret_cast<const u32x4*>(bt + 4 * lane);
  int len = lens[sb];
  const u64 tag = *(const __attribute__((address_space(4))) u64*)(ws + W_EPOCH + kvh * 128) + 1;
  __builtin_amdgcn_sched_barrier(0);
  const unsigned swz = (unsigned)(c ^ ((g & 2) << 2)) * 16;
  {
    const __amdgpu_buffer_rsrc_t qrs = __builtin_amdgcn_make_buffer_rsrc((void*)q, 0, NB * NH * HD * 2, 0x00020000);
    dma(ldsb + L_Q + wave * PIECE, g * 256 + swz, qrs, (wave * NH + kvh * 4) * HD * 2);
  }
  const __amdgpu_buffer_rsrc_t krs = __builtin_amdgcn_make_buffer_rsrc((void*)(kc + kvh * HD), 0, kv_bytes - kvh * HD * 2, 0x00020000);
  const __amdgpu_buffer_rsrc_t vrs = __builtin_amdgcn_make_buffer_rsrc((void*)(vc + kvh * HD), 0, kv_bytes - kvh * HD * 2, 0x00020000);
  const __amdgpu_buffer_rsrc_t wrs = __builtin_amdgcn_make_buffer_rsrc((void*)ws, 0, W_END, 0x00020000);
  const unsigned rowslot = (unsigned)(kvh * 32 + wi) * WGH;
  if (tid == 0) *reinterpret_cast<int*>(lds + L_CNT) = 0;
  unsigned voff = g * TOKB + c * 16, koff = g * TOKB + swz;
  const unsigned wreg = wave * WREG;
  unsigned krd = wreg + (c & 3) * PIECE + (c >> 2) * 256 + ((4 * g) ^ (c & 8)) * 16;
  unsigned qrd = L_Q + (c >> 2) * 256 + ((4 * g) ^ (c & 8)) * 16;
  f32x4 acc[2][8];
#pragma unroll
  for (int t = 0; t < 2; ++t)
#pragma unroll
    for (int d = 0; d < 8; ++d) acc[t][d] = f32x4{0.f, 0.f, 0.f, 0.f};
  float M[2] = {-INFINITY, -INFINITY}, l[2] = {0.f, 0.f};
  unsigned kdst = ldsb + wreg;
  asm volatile("" : "+v"(voff), "+v"(koff), "+v"(krd), "+v"(qrd), "+s"(kdst) : "s"(krs), "s"(vrs), "s"(wrs));
  __builtin_amdgcn_sched_barrier(0);
  len = min(max(len, 0), LK);
  const int np = (len + 15) >> 4;
  int psn = np;
  { const int t = dppi<0x111>(psn); psn += sb >= 1 ? t : 0; }
  { const int t = dppi<0x112>(psn); psn += sb >= 2 ? t : 0; }
  { const int t = dppi<0x114>(psn); psn += sb >= 4 ? t : 0; }
  int ps8 = __builtin_amdgcn_readlane(psn, 7);
  {
    int first = min8(len >> 4);
#pragma unroll
    for (int k = 0; k < 4; ++k) {
      const u64 m = __ballot(tv[k] != t0[k]);
      first = min(first, m ? 4 * (int)__builtin_ctzll(m) + k : NPG);
    }
    *reinterpret_cast<u32x4*>(lds + L_BT + wave * (NPG * 4) + lane * 16) = tv;
    if (lane == 0) *reinterpret_cast<int*>(lds + L_FIRST + wave * 4) = first;
  }
  int uo = wave + (lane & 56), bta = L_BT + sb * (NPG * 4);
  asm volatile("" : "+v"(psn), "+s"(ps8), "+v"(uo), "+v"(bta));
  STAMP(1);
  asm volatile("s_waitcnt vmcnt(0) lgkmcnt(0)\n\ts_barrier" ::: "memory");
  asm volatile("" ::"v"(pf_dummy));
  STAMP(2);
  int Sv, bnd;
  {
    const u32x4 a = *reinterpret_cast<const u32x4*>(lds + L_FIRST), b = *reinterpret_cast<const u32x4*>(lds + L_FIRST + 16);
    Sv = (int)min(min(min(a[0], a[1]), min(a[2], a[3])), min(min(b[0], b[1]), min(b[2], b[3])));
    Sv = Sv < 8 ? 0 : Sv;
    asm volatile("" : "+v"(Sv));
    const int P = ps8 - NB * Sv;
    const float fS = (float)Sv, fP = (float)P;
    const float fn = 288.f * fS * __builtin_amdgcn_rcpf(fmaf(9.f, fS, fmaf(8.f, fP, 1e-3f)));
    const int nsh = min(max((int)(fn + 0.5f), (Sv + 63) >> 6), WGH - ((P + 63) >> 6));
    const bool lo = lane <= nsh;
    const int cnt = lo ? nsh : WGH - nsh, tot = lo ? Sv : P, i = lo ? lane : lane - nsh;
    const int qq = (int)(((lo ? fS : fP) + 0.5f) * __builtin_amdgcn_rcpf((float)max(cnt, 1)));
    bnd = (lo ? 0 : Sv) + i * qq + min(i, tot - qq * cnt);
  }
  const int u0 = __builtin_amdgcn_readlane(bnd, wi), u1 = __builtin_amdgcn_readlane(bnd, wi + 1);
  const int S = __builtin_amdgcn_readfirstlane(Sv);
  const int offn = psn - sb * Sv, offb = offn - np + Sv;
  unsigned so_v;
  int info_v;
  {
    const int u = u0 + uo;
    const bool sh = u < S;
    const bool hit = (u < u1) & (sh ? sb == 0 : (u >= offb) & (u < offn));
    const int j = sh ? u : min(max(S + u - offb, 0), NPG - 1);
    int phys = *reinterpret_cast<const int*>(lds + bta + j * 4);
    asm volatile("" : "+v"(phys));
    so_v = (unsigned)min8u(hit ? (unsigned)phys * PAGEB : OOB);
    info_v = hit ? (sh ? 16 | 8 << 8 : min(16, len - 16 * j) | sb << 8) : 0;
  }
  const int cb = (wi >> 4) * 4 + (wi & 3), ch = (wi & 15) >> 2;

  u32x2 fl;
  unsigned flo;
  const u32x4 tag4 = {(unsigned)tag, (unsigned)(tag >> 32), (unsigned)tag, (unsigned)(tag >> 32)};
  int blo, nseq, clen;
  unsigned pmask;
  unsigned so[NP];

  auto issue_k = [&](auto rdc) __attribute__((always_inline)) {
    constexpr int rd = decltype(rdc)::value;
#pragma unroll
    for (int p = 0; p < NP; ++p) so[p] = __builtin_amdgcn_readlane(so_v, 8 * (NP * rd + p));
    STAMP(3);
    __builtin_amdgcn_s_setprio(1);
    dma_page<0>(kdst, koff, krs, so[0]);
    __builtin_amdgcn_sched_barrier(0);
    if constexpr (rd == 0) {
      const int cofl = __builtin_amdgcn_readlane(offb, cb), cofh = __builtin_amdgcn_readlane(offn, cb);
      const int a0 = bnd, a1 = __builtin_amdgcn_update_dpp(0, bnd, 0x130, 0xf, 0xf, false);
      pmask = (unsigned)__ballot((a1 > a0) & ((a0 < S) | ((a0 < cofh) & (a1 > cofl))));
      const u32x4 sv = {SENT, SENT, SENT, SENT};
      const unsigned pm = pmask >> (4 * wave + (lane >> 5));
      __builtin_amdgcn_raw_buffer_store_b128(sv, wrs, (pm & 1) ? lane * 16 : OOB, W_O + (rowslot + 4 * wave) * (HD * 4), 16);
      __builtin_amdgcn_raw_buffer_store_b128(sv, wrs, (pm & 4) ? lane * 16 + 1024 : OOB, W_O + (rowslot + 4 * wave) * (HD * 4), 16);
    }
    __builtin_amdgcn_sched_barrier(0);
    dma_page<1>(kdst, koff, krs, so[1]);
    dma_page<2>(kdst, koff, krs, so[2]);
    dma_page<3>(kdst, koff, krs, so[3]);
    __builtin_amdgcn_s_setprio(0);
    __builtin_amdgcn_sched_barrier(0);
    STAMP(4);
  };

  auto round = [&](auto rdc, auto ntc) __attribute__((always_inline)) {
    constexpr int rd = decltype(rdc)::value, NT = decltype(ntc)::value;
    int info[NP];
    {
      const int inf = max8(info_v);
#pragma unroll
      for (int p = 0; p < NP; ++p) info[p] = __builtin_amdgcn_readlane(inf, 8 * (NP * rd + p));
    }
    u32x4 qf[NT][4];
    int csq[NT];
#pragma unroll
    for (int t = 0; t < NT; ++t) {
      csq[t] = (blo + 4 * t + (c & 3)) & 7;
#pragma unroll
      for (int s = 0; s < 4; ++s) qf[t][s] = *reinterpret_cast<const u32x4*>(lds + qrd + csq[t] * PIECE + s * 16);
    }
    f32x4 sc[NP][NT];
    float mx[NT];
#pragma unroll
    for (int t = 0; t < NT; ++t) mx[t] = M[t];
#pragma unroll
    for (int p = 0; p < NP; ++p) {
      if (p == 0) wait_vm<rd == 0 ? 14 : 12>();
      if (p == 1) wait_vm<8>();
      if (p == 2) wait_vm<4>();
      if (p == 3) wait_vm<0>();
      if (p == 1 && rd == 0) {
        int old = 0;
        if (lane == 0) old = __hip_atomic_fetch_add(reinterpret_cast<int*>(lds + L_CNT), 1, __ATOMIC_RELAXED, __HIP_MEMORY_SCOPE_WORKGROUP);
        if (__builtin_amdgcn_readfirstlane(old) == NW - 1)
          __builtin_amdgcn_raw_buffer_store_b128(tag4, wrs, lane < 8 ? lane * 16 : OOB, W_FLAG + (kvh * 32 + wi) * 128, 16);
      }
      u32x4 kf[4];
#pragma unroll
      for (int s = 0; s < 4; ++s) kf[s] = *reinterpret_cast<const u32x4*>(lds + krd + p * KPAGE + s * 16);
      const int nv = info[p] & 255, sq = info[p] >> 8;
#pragma unroll
      for (int t = 0; t < NT; ++t) {
        f32x4 a = {0.f, 0.f, 0.f, 0.f};
#pragma unroll
        for (int s = 0; s < 4; ++s) a = mfma(kf[s], qf[t][s], a);
        const bool cv = (sq == 8) | (sq == csq[t]);
#pragma unroll
        for (int r = 0; r < 4; ++r) {
          a[r] = (cv & (4 * r + g < nv)) ? a[r] : -INFINITY;
          mx[t] = fmaxf(mx[t], a[r]);
        }
        sc[p][t] = a;
      }
    }
    __builtin_amdgcn_sched_barrier(0);
    u32x4 vf[NP][4];
#pragma unroll
    for (int p = 0; p < NP; ++p)
#pragma unroll
      for (int i = 0; i < 4; ++i) vf[p][i] = __builtin_amdgcn_raw_buffer_load_b128(vrs, voff, so[p] + i * 4 * TOKB, 2);
    fl = __builtin_amdgcn_raw_buffer_load_b64(wrs, flo, 0, 16);
    STAMP(5);
    __builtin_amdgcn_sched_barrier(0);
    u32x4 pf[NP / 2][NT];
#pragma unroll
    for (int t = 0; t < NT; ++t) {
      const float mn = xg_max(mx[t]);
      const float ms = (mn == -INFINITY) ? 0.f : mn * C2;
      float ls = 0.f;
      if constexpr (rd != 0) {
        const float alpha = __builtin_amdgcn_exp2f(M[t] * C2 - ms);
        ls = l[t] * alpha;
#pragma unroll
        for (int d = 0; d < 8; ++d) acc[t][d] *= alpha;
      }
      M[t] = mn;
#pragma unroll
      for (int p = 0; p < NP; ++p) {
        float e[4];
#pragma unroll
        for (int r = 0; r < 4; ++r) { e[r] = __builtin_amdgcn_exp2f(fmaf(sc[p][t][r], C2, -ms)); ls += e[r]; }
        pf[p >> 1][t][(p & 1) * 2] = pk_bf16(e[0], e[1]);
        pf[p >> 1][t][(p & 1) * 2 + 1] = pk_bf16(e[2], e[3]);
      }
      l[t] = ls;
      const float lw = xg_sum(ls);
      if (g < 2) *reinterpret_cast<float*>(lds + (g ? L_L : L_M) + ((16 * t + c) * NW + wave) * 4) = g ? lw : mn;
    }
    STAMP(6);
    __builtin_amdgcn_sched_barrier(0);
#pragma unroll
    for (int pp = 0; pp < NP / 2; ++pp) {
#pragma unroll
      for (int e = 0; e < 4; ++e) {
        u32x4 lo, hi;
#pragma unroll
        for (int h = 0; h < 2; ++h) {
          const u32x4* v = vf[2 * pp + h];
          lo[2 * h] = __builtin_amdgcn_perm(v[1][e], v[0][e], 0x05040100u);
          lo[2 * h + 1] = __builtin_amdgcn_perm(v[3][e], v[2][e], 0x05040100u);
          hi[2 * h] = __builtin_amdgcn_perm(v[1][e], v[0][e], 0x07060302u);
          hi[2 * h + 1] = __builtin_amdgcn_perm(v[3][e], v[2][e], 0x07060302u);
        }
#pragma unroll
        for (int t = 0; t < NT; ++t) {
          acc[t][2 * e] = mfma(lo, pf[pp][t], acc[t][2 * e]);
          acc[t][2 * e + 1] = mfma(hi, pf[pp][t], acc[t][2 * e + 1]);
        }
      }
      __builtin_amdgcn_sched_barrier(0);
    }
  };
  auto store_o = [&](auto ntc) __attribute__((always_inline)) {
    constexpr int NT = decltype(ntc)::value;
    STAMP(7);
#pragma unroll
    for (int t = 0; t < NT; ++t)
      if (4 * t + (c & 3) < nseq) {
#pragma unroll
        for (int d = 0; d < 8; ++d) *reinterpret_cast<f32x4*>(lds + wreg + (16 * t + c) * OROW + g * 128 + d * 16) = acc[t][d];
      }
  };
  using I0 = std::integral_constant<int, 0>;
  using I1 = std::integral_constant<int, 1>;
  using I2 = std::integral_constant<int, 2>;
  issue_k(I0{});
  {
    const unsigned sm = (unsigned)__ballot((u1 > u0) & ((u0 < S) | ((u0 < offn) & (u1 > offb)))) & 255u;
    blo = sm ? __builtin_ctz(sm) : 0;
    nseq = __builtin_popcount(sm);
    clen = __builtin_amdgcn_readlane(len, cb);
    asm volatile("" : "+s"(blo), "+s"(nseq), "+s"(clen), "+s"(pmask));
    const bool need = (wave == NW - 1) & (lane < 32) & ((wi == 0) | ((unsigned)((lane >> 4) * 4 + (lane & 3) - blo) < (unsigned)nseq));
    flo = need ? W_FLAG + (kvh * 32 + lane) * 128 : OOB;
  }
  if (nseq <= 4 && u1 - u0 <= NW * NP) {
    round(I0{}, I1{});
    store_o(I1{});
  } else {
    round(I0{}, I2{});
    if (__builtin_expect(u1 - u0 > NW * NP, 0)) {
      issue_k(I1{});
      round(I1{}, I2{});
    }
    store_o(I2{});
  }
  const int x = tid & 31, k0 = tid >> 5;
#pragma nounroll
  for (unsigned it = 0; it < SPIN; ++it) {
    const bool ok = (flo == OOB) | ((fl[0] == (unsigned)tag) & (fl[1] == (unsigned)(tag >> 32)));
    if (__all(ok)) break;
    asm volatile("s_sleep 2" ::: "memory");
    fl = __builtin_amdgcn_raw_buffer_load_b64(wrs, flo, 0, 16);
  }
  STAMP(8);
  __syncthreads();
  STAMP(9);
  for (int k = k0; k < 4 * nseq; k += NW * 2) {
    const int lr = (k & 16) + (k & 3) * 4 + ((k >> 2) & 3), b = blo + (k >> 2), row = (b >> 2) * 16 + (k & 3) * 4 + (b & 3);
    const f32x4 ma = *reinterpret_cast<const f32x4*>(lds + L_M + lr * NW * 4), mb = *reinterpret_cast<const f32x4*>(lds + L_M + lr * NW * 4 + 16);
    const f32x4 la = *reinterpret_cast<const f32x4*>(lds + L_L + lr * NW * 4), lb = *reinterpret_cast<const f32x4*>(lds + L_L + lr * NW * 4 + 16);
    f32x4 ow[NW];
#pragma unroll
    for (int w = 0; w < NW; ++w) ow[w] = *reinterpret_cast<const f32x4*>(lds + w * WREG + lr * OROW + x * 16);
    __builtin_amdgcn_sched_barrier(0);
    const float Mx = fmaxf(fmaxf(fmaxf(ma[0], ma[1]), fmaxf(ma[2], ma[3])), fmaxf(fmaxf(mb[0], mb[1]), fmaxf(mb[2], mb[3])));
    const float ms = (Mx == -INFINITY) ? 0.f : Mx * C2;
    float f[NW];
#pragma unroll
    for (int w = 0; w < NW; ++w) f[w] = __builtin_amdgcn_exp2f(fmaf(w < 4 ? ma[w & 3] : mb[w & 3], C2, -ms));
    const float L = (fmaf(f[0], la[0], f[1] * la[1]) + fmaf(f[2], la[2], f[3] * la[3])) + (fmaf(f[4], lb[0], f[5] * lb[1]) + fmaf(f[6], lb[2], f[7] * lb[3]));
    const f32x4 o = ((f[0] * ow[0] + f[1] * ow[1]) + (f[2] * ow[2] + f[3] * ow[3])) + ((f[4] * ow[4] + f[5] * ow[5]) + (f[6] * ow[6] + f[7] * ow[7]));
    const unsigned slot = (unsigned)(kvh * 32 + row) * WGH + wi;
    const unsigned e0 = (__float_as_uint(o[0]) & ~1u) | ((__float_as_uint(Mx) >> x) & 1u);
    const unsigned e1 = (__float_as_uint(o[1]) & ~1u) | ((__float_as_uint(L) >> x) & 1u);
    __builtin_amdgcn_raw_buffer_store_b128(u32x4{canon(e0), canon(e1), canon(__float_as_uint(o[2])), canon(__float_as_uint(o[3]))}, wrs,
                                           W_O + slot * (HD * 4) + x * 16, 0, 16);
  }
  STAMP(10);
  if (wave == NW - 1) {
    if (wi == 0) __builtin_amdgcn_raw_buffer_store_b128(tag4, wrs, lane < 8 ? lane * 16 : OOB, W_EPOCH + kvh * 128, 16);
    constexpr int NL = 6;
    const int hw = lane >> 5;
    float Mx = -INFINITY, L = 0.f;
    f32x4 o = {0.f, 0.f, 0.f, 0.f};
    unsigned m = pmask;
    while (m) {
      unsigned vo[NL], todo = 0;
      u32x4 ov[NL];
      float mk[NL], lk[NL];
#pragma unroll
      for (int k = 0; k < NL; ++k) {
        todo |= m ? 1u << k : 0u;
        const unsigned s0 = m ? (rowslot + __builtin_ctz(m)) * (HD * 4) + W_O : OOB;
        m &= m - 1;
        const unsigned s1 = m ? (rowslot + __builtin_ctz(m)) * (HD * 4) + W_O : OOB;
        m &= m - 1;
        vo[k] = (hw ? s1 : s0) + x * 16;
        ov[k] = u32x4{0u, 0u, 0u, 0u};
        mk[k] = -INFINITY;
        lk[k] = 0.f;
      }
#pragma nounroll
      for (unsigned it = 0; it < SPIN && todo; ++it) {
#pragma unroll
        for (int k = 0; k < NL; ++k)
          if (todo & (1u << k)) ov[k] = __builtin_amdgcn_raw_buffer_load_b128(wrs, vo[k], 0, 16);
#pragma unroll
        for (int k = 0; k < NL; ++k)
          if (todo & (1u << k)) {
            if (__ballot(max(max(ov[k][0], ov[k][1]), max(ov[k][2], ov[k][3])) == SENT) == 0) {
              const u64 bm = __ballot(ov[k][0] & 1), bl = __ballot(ov[k][1] & 1);
              mk[k] = vo[k] >= OOB ? -INFINITY : __uint_as_float(hw ? (unsigned)(bm >> 32) : (unsigned)bm);
              lk[k] = __uint_as_float(hw ? (unsigned)(bl >> 32) : (unsigned)bl);
              todo &= ~(1u << k);
            }
          }
        asm volatile("" ::: "memory");
      }
      STAMP(12);
      float mn = Mx;
#pragma unroll
      for (int k = 0; k < NL; ++k) mn = fmaxf(mn, mk[k]);
      {
        auto s = __builtin_amdgcn_permlane32_swap(__float_as_uint(mn), __float_as_uint(mn), false, false);
        mn = fmaxf(__uint_as_float(s[0]), __uint_as_float(s[1]));
      }
      const float ms = (mn == -INFINITY) ? 0.f : mn * C2;
      const float alpha = __builtin_amdgcn_exp2f(Mx * C2 - ms);
      Mx = mn;
      L *= alpha;
      o *= alpha;
#pragma unroll
      for (int k = 0; k < NL; ++k) {
        const float w = __builtin_amdgcn_exp2f(fmaf(mk[k], C2, -ms));
        L = fmaf(w, lk[k], L);
        o += w * __builtin_bit_cast(f32x4, ov[k]);
      }
    }
    {
      auto s = __builtin_amdgcn_permlane32_swap(__float_as_uint(L), __float_as_uint(L), false, false);
      L = __uint_as_float(s[0]) + __uint_as_float(s[1]);
    }
#pragma unroll
    for (int r = 0; r < 4; ++r) {
      auto s = __builtin_amdgcn_permlane32_swap(__float_as_uint(o[r]), __float_as_uint(o[r]), false, false);
      o[r] = __uint_as_float(s[0]) + __uint_as_float(s[1]);
    }
    const int d0 = 32 * (x >> 3) + (x & 7);
    const int lb = clen;
    if (lb < LK && !(Mx * SCALE >= MASKV + 256.f)) {
      const float Mn = Mx * SCALE, Mt = fmaxf(Mn, MASKV);
      const float wv = (L > 0.f) ? __expf(Mn - Mt) : 0.f, wm = __expf(MASKV - Mt);
      f32x4 sv = {0.f, 0.f, 0.f, 0.f};
      for (int pos = lb; pos < LK; ++pos) {
        const int phys = *reinterpret_cast<const int*>(lds + L_BT + (cb * NPG + (pos >> 4)) * 4);
        const unsigned short* vp = vc + ((size_t)phys * PS + (pos & 15)) * (NKV * HD) + kvh * HD + d0;
#pragma unroll
        for (int r = 0; r < 4; ++r) sv[r] += __uint_as_float((unsigned)vp[8 * r] << 16);
      }
      o = o * wv + sv * wm;
      L = L * wv + wm * (float)(LK - lb);
    }
    const float inv = __builtin_amdgcn_rcpf(L);
    STAMP(13);
    unsigned short* op = out + (cb * NH + kvh * 4 + ch) * HD + d0 + 16 * hw;
    op[0] = __builtin_bit_cast(unsigned short, (__bf16)((hw ? o[2] : o[0]) * inv));
    op[8] = __builtin_bit_cast(unsigned short, (__bf16)((hw ? o[3] : o[1]) * inv));
  }
  STAMP_FLUSH(ws, blockIdx.x * NW + wave);
}

}

extern "C" void kernel_launch(void* const* d_in, const int* in_sizes, int n_in, void* d_out, int out_size, void* d_ws, size_t ws_size,
                              hipStream_t stream) {
  attn_kernel<<<NKV * WGH, NW * 64, 0, stream>>>((const unsigned short*)d_in[0], (const unsigned short*)d_in[1], (const unsigned short*)d_in[2],
                                                 (const int*)d_in[3], (const int*)d_in[4], (unsigned short*)d_out, (unsigned char*)d_ws,
                                                 (unsigned)in_sizes[1] * 2u);
}
```

```cpp
#define STAMPS
#define DIAG_I 7
#define DIAG_MIN 0
#define DIAG_HEAD 1
#include <hip/hip_runtime.h>
#include <hip/hip_bf16.h>
#include <stdint.h>
#include <type_traits>

typedef __attribute__((ext_vector_type(8))) __bf16 bf16x8;
typedef __attribute__((ext_vector_type(4))) float f32x4;
typedef __attribute__((ext_vector_type(2))) float f32x2;
typedef __attribute__((ext_vector_type(4))) unsigned u32x4;
typedef __attribute__((ext_vector_type(2))) unsigned u32x2;
typedef unsigned long long u64;

#include <cstdio>
#include <vector>
#include <algorithm>
#include <cstdlib>
constexpr int NST = 12;
constexpr size_t ST_OFF = (size_t)128 << 20;
#define STAMP(i) do { __builtin_amdgcn_sched_barrier(0); st_[i] = __builtin_amdgcn_s_memrealtime(); __builtin_amdgcn_sched_barrier(0); } while (0)
#define STAMP_DECL unsigned long long st_[NST] = {}
#define STAMP_FLUSH(ws, slot) do { if ((threadIdx.x & 63) == 0) { unsigned long long* p_ = (unsigned long long*)((char*)(ws) + ST_OFF) + (size_t)(slot) * NST; for (int i_ = 0; i_ < NST; ++i_) p_[i_] = st_[i_]; } } while (0)
static void dump_stamps(void* ws) {
  const int n = 2048;
  std::vector<unsigned long long> h((size_t)n * NST);
  (void)hipMemcpy(h.data(), (char*)ws + ST_OFF, h.size() * 8, hipMemcpyDeviceToHost);
  unsigned long long base = ~0ull;
  for (int w = 0; w < 2048; ++w) base = std::min(base, h[(size_t)w * NST]);
  for (int i = 0; i < NST; ++i) {
    std::vector<double> v;
    for (int w = 0; w < 2048; ++w) { unsigned long long t = h[(size_t)w * NST + i]; if (t >= base && t - base < 100000) v.push_back((t - base) * 0.01); }
    if (v.empty()) continue;
    std::sort(v.begin(), v.end());
    printf("  st%-2d n=%4zu min %6.2f p10 %6.2f med %6.2f p90 %6.2f max %6.2f\n", i, v.size(), v[0], v[v.size() / 10], v[v.size() / 2], v[v.size() * 9 / 10], v.back());
  }
  if (getenv("DETAIL")) {
    const int si = atoi(getenv("DETAIL"));
    printf("  per-WG max of st%d (rows: head, cols: wi); last col: head's first start\n", si);
    for (int k = 0; k < 8; ++k) {
      unsigned long long s0 = ~0ull;
      for (int wi = 0; wi < 32; ++wi) {
        unsigned long long m = 0;
        for (int w = 0; w < 8; ++w) { m = std::max(m, h[(size_t)((wi * 8 + k) * 8 + w) * NST + si]); s0 = std::min(s0, h[(size_t)((wi * 8 + k) * 8 + w) * NST]); }
        printf(" %4.1f", (m - base) * 0.01);
      }
      printf(" | %4.2f\n", (s0 - base) * 0.01);
    }
    printf("  per-wave (WG 80..83) stamps:\n");
    for (int b = 80; b < 84; ++b) for (int w = 0; w < 8; ++w) { printf("   b%d w%d:", b, w); for (int i = 0; i < NST; ++i) printf(" %5.2f", (h[(size_t)(b * 8 + w) * NST + i] - base) * 0.01); printf("\n"); }
  }
}

namespace {
constexpr int NB = 8, NH = 32, NKV = 8, HD = 128, NPG = 256, PS = 16, LK = NPG * PS;
constexpr int WGH = 32;
constexpr int NW = 8;
constexpr int NP = 4;
constexpr float SCALE = 0.08838834764831845f;
constexpr float C2 = SCALE * 1.4426950408889634f;
constexpr float MASKV = -998244352.0f;
constexpr unsigned TOKB = NKV * HD * 2, PAGEB = PS * TOKB;
constexpr unsigned OOB = 0x80000000u;
constexpr unsigned SENT = 0xFFFFFFFFu;
constexpr unsigned SPIN = 1u << 18;
constexpr unsigned CODE_PF = 14 * 1024;

constexpr int PIECE = 1040, KPAGE = 4 * PIECE, OROW = 528, WREG = 32 * OROW;
constexpr int L_Q = NW * WREG, L_BT = L_Q + NB * PIECE, L_ML = L_BT + NB * NPG * 4, L_FIRST = L_ML + NW * 32 * 8, L_CNT = L_FIRST + 32, L_END = L_CNT + 16;
static_assert(NP * KPAGE <= WREG && L_END <= 160 * 1024, "lds");

constexpr unsigned W_EPOCH = 0;
constexpr unsigned W_FLAG = NKV * 128;
constexpr unsigned W_O = W_FLAG + NKV * 32 * 128;
constexpr unsigned W_END = W_O + NKV * 32 * WGH * HD * 4;

__device__ __forceinline__ float xg_max(float x) {
  auto s = __builtin_amdgcn_permlane16_swap(__float_as_uint(x), __float_as_uint(x), false, false);
  x = fmaxf(__uint_as_float(s[0]), __uint_as_float(s[1]));
  auto t = __builtin_amdgcn_permlane32_swap(__float_as_uint(x), __float_as_uint(x), false, false);
  return fmaxf(__uint_as_float(t[0]), __uint_as_float(t[1]));
}
__device__ __forceinline__ float xg_sum(float x) {
  auto s = __builtin_amdgcn_permlane16_swap(__float_as_uint(x), __float_as_uint(x), false, false);
  x = __uint_as_float(s[0]) + __uint_as_float(s[1]);
  auto t = __builtin_amdgcn_permlane32_swap(__float_as_uint(x), __float_as_uint(x), false, false);
  return __uint_as_float(t[0]) + __uint_as_float(t[1]);
}
template <int CTRL> __device__ __forceinline__ float dpp(float x) {
  return __builtin_bit_cast(float, __builtin_amdgcn_mov_dpp(__builtin_bit_cast(int, x), CTRL, 0xf, 0xf, true));
}
template <int CTRL> __device__ __forceinline__ int dppi(int x) { return __builtin_amdgcn_mov_dpp(x, CTRL, 0xf, 0xf, true); }
__device__ __forceinline__ int min8(int x) { x = min(x, dppi<0xB1>(x)); x = min(x, dppi<0x4E>(x)); return min(x, dppi<0x141>(x)); }
__device__ __forceinline__ int max8(int x) { x = max(x, dppi<0xB1>(x)); x = max(x, dppi<0x4E>(x)); return max(x, dppi<0x141>(x)); }
__device__ __forceinline__ unsigned min8u(unsigned x) {
  x = min(x, (unsigned)dppi<0xB1>((int)x)); x = min(x, (unsigned)dppi<0x4E>((int)x)); return min(x, (unsigned)dppi<0x141>((int)x));
}
__device__ __forceinline__ float row_max(float x) {
  x = fmaxf(x, dpp<0xB1>(x)); x = fmaxf(x, dpp<0x4E>(x)); x = fmaxf(x, dpp<0x141>(x)); return fmaxf(x, dpp<0x128>(x));
}
__device__ __forceinline__ float row_sum(float x) {
  x += dpp<0xB1>(x); x += dpp<0x4E>(x); x += dpp<0x141>(x); return x + dpp<0x128>(x);
}
__device__ __forceinline__ unsigned pk_bf16(float lo, float hi) {
  typedef __attribute__((ext_vector_type(2))) __bf16 bf16x2;
  bf16x2 v = {(__bf16)lo, (__bf16)hi};
  return __builtin_bit_cast(unsigned, v);
}
__device__ __forceinline__ f32x4 mfma(u32x4 a, u32x4 b, f32x4 c) {
  return __builtin_amdgcn_mfma_f32_16x16x32_bf16(__builtin_bit_cast(bf16x8, a), __builtin_bit_cast(bf16x8, b), c, 0, 0, 0);
}
__device__ __forceinline__ unsigned canon(unsigned u) { return u == SENT ? 0x7FFFFFFFu : u; }
template <int N> __device__ __forceinline__ void wait_vm() { asm volatile("s_waitcnt vmcnt(%0)" ::"n"(N) : "memory"); }
template <int LO, int SO> __device__ __forceinline__ void dma_nt(unsigned lds, unsigned voff, __amdgpu_buffer_rsrc_t rs, unsigned soff) {
  unsigned t;
  asm volatile("s_add_u32 m0, %1, %5\n\ts_add_u32 %0, %4, %6\n\tbuffer_load_dwordx4 %2, %3, %0 offen nt lds"
               : "=&s"(t) : "s"(lds), "v"(voff), "s"(rs), "s"(soff), "n"(LO), "n"(SO) : "memory", "scc");
}
template <int P> __device__ __forceinline__ void dma_page(unsigned lds, unsigned voff, __amdgpu_buffer_rsrc_t rs, unsigned soff) {
  dma_nt<P * KPAGE, 0>(lds, voff, rs, soff);
  dma_nt<P * KPAGE + PIECE, 4 * TOKB>(lds, voff, rs, soff);
  dma_nt<P * KPAGE + 2 * PIECE, 8 * TOKB>(lds, voff, rs, soff);
  dma_nt<P * KPAGE + 3 * PIECE, 12 * TOKB>(lds, voff, rs, soff);
}
__device__ __forceinline__ void dma(unsigned lds, unsigned voff, __amdgpu_buffer_rsrc_t rs, unsigned soff) {
  asm volatile("s_mov_b32 m0, %0\n\ts_nop 0\n\tbuffer_load_dwordx4 %1, %2, %3 offen lds" ::"s"(lds), "v"(voff), "s"(rs), "s"(soff) : "memory");
}

__global__ __launch_bounds__(NW * 64, 2) void attn_kernel(const unsigned short* __restrict__ q, const unsigned short* __restrict__ kc,
                                                          const unsigned short* __restrict__ vc, const int* __restrict__ bt,
                                                          const int* __restrict__ lens, unsigned short* __restrict__ out,
                                                          unsigned char* __restrict__ ws, unsigned kv_bytes) {
  __shared__ __attribute__((aligned(16))) unsigned char lds[L_END];
  STAMP_DECL;
  STAMP(0);
  unsigned pf_dummy;
  {
    u64 pc;
    asm volatile("s_getpc_b64 %1\n\tglobal_load_dword %0, %2, %1" : "=&v"(pf_dummy), "=&s"(pc) : "v"(min(threadIdx.x * 128u, CODE_PF - 128u)));
  }
  const int tid = threadIdx.x, lane = tid & 63, wave = __builtin_amdgcn_readfirstlane(tid >> 6);
  const int g = lane >> 4, c = lane & 15;
  const int kvh = blockIdx.x & (NKV - 1), wi = blockIdx.x >> 3;
  const unsigned ldsb = (unsigned)(size_t)lds;
  asm volatile("" ::"s"(q), "s"(kc), "s"(vc), "s"(bt), "s"(lens), "s"(out), "s"(ws), "s"(kv_bytes));

  const int sb = lane & 7;
  const u32x4 tv = *reinterpret_cast<const u32x4*>(bt + wave * NPG + 4 * lane);
  const u32x4 t0 = *reinterpret_cast<const u32x4*>(bt + 4 * lane);
  int len = lens[sb];
  const u64 tag = *(const __attribute__((address_space(4))) u64*)(ws + W_EPOCH + kvh * 128) + 1;
  __builtin_amdgcn_sched_barrier(0);
  const unsigned swz = (unsigned)(c ^ ((g & 2) << 2)) * 16;
  {
    const __amdgpu_buffer_rsrc_t qrs = __builtin_amdgcn_make_buffer_rsrc((void*)q, 0, NB * NH * HD * 2, 0x00020000);
    dma(ldsb + L_Q + wave * PIECE, g * 256 + swz, qrs, (wave * NH + kvh * 4) * HD * 2);
  }
  const __amdgpu_buffer_rsrc_t krs = __builtin_amdgcn_make_buffer_rsrc((void*)(kc + kvh * HD), 0, kv_bytes - kvh * HD * 2, 0x00020000);
  const __amdgpu_buffer_rsrc_t vrs = __builtin_amdgcn_make_buffer_rsrc((void*)(vc + kvh * HD), 0, kv_bytes - kvh * HD * 2, 0x00020000);
  const __amdgpu_buffer_rsrc_t wrs = __builtin_amdgcn_make_buffer_rsrc((void*)ws, 0, W_END, 0x00020000);
  const unsigned rowslot = (unsigned)(kvh * 32 + wi) * WGH;
  if (tid == 0) *reinterpret_cast<int*>(lds + L_CNT) = 0;
  unsigned voff = g * TOKB + c * 16, koff = g * TOKB + swz;
  const unsigned wreg = wave * WREG;
  unsigned krd = wreg + (c & 3) * PIECE + (c >> 2) * 256 + ((4 * g) ^ (c & 8)) * 16;
  unsigned qrd = L_Q + (c & 3) * PIECE + (c >> 2) * 256 + ((4 * g) ^ (c & 8)) * 16;
  f32x4 acc[2][8];
#pragma unroll
  for (int t = 0; t < 2; ++t)
#pragma unroll
    for (int d = 0; d < 8; ++d) acc[t][d] = f32x4{0.f, 0.f, 0.f, 0.f};
  float M[2] = {-INFINITY, -INFINITY}, l[2] = {0.f, 0.f};
  unsigned kdst = ldsb + wreg;
  asm volatile("" : "+v"(voff), "+v"(koff), "+v"(krd), "+v"(qrd), "+s"(kdst) : "s"(krs), "s"(vrs), "s"(wrs));
  __builtin_amdgcn_sched_barrier(0);
  len = min(max(len, 0), LK);
  const int np = (len + 15) >> 4;
  int psn = np;
  { const int t = dppi<0x111>(psn); psn += sb >= 1 ? t : 0; }
  { const int t = dppi<0x112>(psn); psn += sb >= 2 ? t : 0; }
  { const int t = dppi<0x114>(psn); psn += sb >= 4 ? t : 0; }
  int ps8 = __builtin_amdgcn_readlane(psn, 7);
  {
    int first = min8(len >> 4);
#pragma unroll
    for (int k = 0; k < 4; ++k) {
      const u64 m = __ballot(tv[k] != t0[k]);
      first = min(first, m ? 4 * (int)__builtin_ctzll(m) + k : NPG);
    }
    *reinterpret_cast<u32x4*>(lds + L_BT + wave * (NPG * 4) + lane * 16) = tv;
    if (lane == 0) *reinterpret_cast<int*>(lds + L_FIRST + wave * 4) = first;
  }
  int uo = wave + (lane & 56), bta = L_BT + sb * (NPG * 4);
  asm volatile("" : "+v"(psn), "+s"(ps8), "+v"(uo), "+v"(bta));
  STAMP(1);
  asm volatile("s_waitcnt vmcnt(0) lgkmcnt(0)\n\ts_barrier" ::: "memory");
  asm volatile("" ::"v"(pf_dummy));
  STAMP(2);
  int Sv, bnd;
  {
    const u32x4 a = *reinterpret_cast<const u32x4*>(lds + L_FIRST), b = *reinterpret_cast<const u32x4*>(lds + L_FIRST + 16);
    Sv = (int)min(min(min(a[0], a[1]), min(a[2], a[3])), min(min(b[0], b[1]), min(b[2], b[3])));
    Sv = Sv < 8 ? 0 : Sv;
    asm volatile("" : "+v"(Sv));
    const int P = ps8 - NB * Sv;
    const float fS = (float)Sv, fP = (float)P;
    const float fn = 288.f * fS * __builtin_amdgcn_rcpf(fmaf(9.f, fS, fmaf(8.f, fP, 1e-3f)));
    const int nsh = min(max((int)(fn + 0.5f), (Sv + 63) >> 6), WGH - ((P + 63) >> 6));
    const bool lo = lane <= nsh;
    const int cnt = lo ? nsh : WGH - nsh, tot = lo ? Sv : P, i = lo ? lane : lane - nsh;
    const int qq = (int)(((lo ? fS : fP) + 0.5f) * __builtin_amdgcn_rcpf((float)max(cnt, 1)));
    bnd = (lo ? 0 : Sv) + i * qq + min(i, tot - qq * cnt);
  }
  const int u0 = __builtin_amdgcn_readlane(bnd, wi), u1 = __builtin_amdgcn_readlane(bnd, wi + 1);
  const int S = __builtin_amdgcn_readfirstlane(Sv);
  const int offn = psn - sb * Sv, offb = offn - np + Sv;
  unsigned so_v;
  int info_v;
  {
    const int u = u0 + uo;
    const bool sh = u < S;
    const bool hit = (u < u1) & (sh ? sb == 0 : (u >= offb) & (u < offn));
    const int j = sh ? u : min(max(S + u - offb, 0), NPG - 1);
    int phys = *reinterpret_cast<const int*>(lds + bta + j * 4);
    asm volatile("" : "+v"(phys));
    so_v = (unsigned)min8u(hit ? (unsigned)phys * PAGEB : OOB);
    info_v = hit ? (sh ? 16 | 8 << 8 : min(16, len - 16 * j) | sb << 8) : 0;
  }
  const int cb = (wi >> 4) * 4 + (wi & 3), ch = (wi & 15) >> 2;

  u32x2 fl;
  unsigned flo;
  const u32x4 tag4 = {(unsigned)tag, (unsigned)(tag >> 32), (unsigned)tag, (unsigned)(tag >> 32)};
  int blo, nseq, clen;
  unsigned pmask;

  auto round = [&](auto rdc) __attribute__((always_inline)) {
    constexpr int rd = decltype(rdc)::value;
    unsigned so[NP];
#pragma unroll
    for (int p = 0; p < NP; ++p) so[p] = __builtin_amdgcn_readlane(so_v, 8 * (NP * rd + p));
    STAMP(3);
    __builtin_amdgcn_s_setprio(1);
    dma_page<0>(kdst, koff, krs, so[0]);
    __builtin_amdgcn_sched_barrier(0);
    if constexpr (rd == 0) {
      const int cofl = __builtin_amdgcn_readlane(offb, cb), cofh = __builtin_amdgcn_readlane(offn, cb);
      const int a0 = bnd, a1 = __builtin_amdgcn_update_dpp(0, bnd, 0x130, 0xf, 0xf, false);
      pmask = (unsigned)__ballot((a1 > a0) & ((a0 < S) | ((a0 < cofh) & (a1 > cofl))));
      const u32x4 sv = {SENT, SENT, SENT, SENT};
      const unsigned pm = pmask >> (4 * wave + (lane >> 5));
      __builtin_amdgcn_raw_buffer_store_b128(sv, wrs, (pm & 1) ? lane * 16 : OOB, W_O + (rowslot + 4 * wave) * (HD * 4), 16);
      __builtin_amdgcn_raw_buffer_store_b128(sv, wrs, (pm & 4) ? lane * 16 + 1024 : OOB, W_O + (rowslot + 4 * wave) * (HD * 4), 16);
    }
    __builtin_amdgcn_sched_barrier(0);
    dma_page<1>(kdst, koff, krs, so[1]);
    dma_page<2>(kdst, koff, krs, so[2]);
    dma_page<3>(kdst, koff, krs, so[3]);
    __builtin_amdgcn_s_setprio(0);
    __builtin_amdgcn_sched_barrier(0);
    STAMP(4);
    int info[NP];
    {
      const int inf = max8(info_v);
#pragma unroll
      for (int p = 0; p < NP; ++p) info[p] = __builtin_amdgcn_readlane(inf, 8 * (NP * rd + p));
    }
    if constexpr (rd == 0) {
      const unsigned sm = (unsigned)__ballot((u1 > u0) & ((u0 < S) | ((u0 < offn) & (u1 > offb)))) & 255u;
      blo = sm ? __builtin_ctz(sm) : 0;
      nseq = __builtin_popcount(sm);
      clen = __builtin_amdgcn_readlane(len, cb);
      asm volatile("" : "+s"(blo), "+s"(nseq), "+s"(clen), "+s"(pmask));
      const bool need = (wave == NW - 1) & (lane < 32) & ((wi == 0) | ((unsigned)((lane >> 4) * 4 + (lane & 3) - blo) < (unsigned)nseq));
      flo = need ? W_FLAG + (kvh * 32 + lane) * 128 : OOB;
    }
    u32x4 qf[2][4];
#pragma unroll
    for (int t = 0; t < 2; ++t)
#pragma unroll
      for (int s = 0; s < 4; ++s) qf[t][s] = *reinterpret_cast<const u32x4*>(lds + qrd + t * 4 * PIECE + s * 16);

    u32x4 vf[NP][4];
    f32x4 sc[NP][2];
    float mx[2] = {M[0], M[1]};
#pragma unroll
    for (int p = 0; p < NP; ++p) {
      if (p == 0) wait_vm<rd == 0 ? 14 : 12>();
      if (p == 1) wait_vm<8>();
      if (p == 2) wait_vm<4>();
      if (p == 3) wait_vm<0>();
      if (p == 1 && rd == 0) {
        int old = 0;
        if (lane == 0) old = __hip_atomic_fetch_add(reinterpret_cast<int*>(lds + L_CNT), 1, __ATOMIC_RELAXED, __HIP_MEMORY_SCOPE_WORKGROUP);
        if (__builtin_amdgcn_readfirstlane(old) == NW - 1)
          __builtin_amdgcn_raw_buffer_store_b128(tag4, wrs, lane < 8 ? lane * 16 : OOB, W_FLAG + (kvh * 32 + wi) * 128, 16);
      }
      u32x4 kf[4];
#pragma unroll
      for (int s = 0; s < 4; ++s) kf[s] = *reinterpret_cast<const u32x4*>(lds + krd + p * KPAGE + s * 16);
      const int nv = info[p] & 255, sq = info[p] >> 8;
#pragma unroll
      for (int t = 0; t < 2; ++t) {
        f32x4 a = {0.f, 0.f, 0.f, 0.f};
#pragma unroll
        for (int s = 0; s < 4; ++s) a = mfma(kf[s], qf[t][s], a);
        const bool cv = (sq == 8) | (sq == 4 * t + (c & 3));
#pragma unroll
        for (int r = 0; r < 4; ++r) {
          a[r] = (cv & (4 * r + g < nv)) ? a[r] : -INFINITY;
          mx[t] = fmaxf(mx[t], a[r]);
        }
        sc[p][t] = a;
      }
    }
    __builtin_amdgcn_sched_barrier(0);
#pragma unroll
    for (int p = 0; p < NP; ++p)
#pragma unroll
      for (int i = 0; i < 4; ++i) vf[p][i] = __builtin_amdgcn_raw_buffer_load_b128(vrs, voff, so[p] + i * 4 * TOKB, 2);
    fl = __builtin_amdgcn_raw_buffer_load_b64(wrs, flo, 0, 16);
    STAMP(5);
    __builtin_amdgcn_sched_barrier(0);
    u32x4 pf[NP / 2][2];
#pragma unroll
    for (int t = 0; t < 2; ++t) {
      const float mn = xg_max(mx[t]);
      const float ms = (mn == -INFINITY) ? 0.f : mn * C2;
      float ls = 0.f;
      if constexpr (rd != 0) {
        const float alpha = __builtin_amdgcn_exp2f(M[t] * C2 - ms);
        ls = l[t] * alpha;
#pragma unroll
        for (int d = 0; d < 8; ++d) acc[t][d] *= alpha;
      }
      M[t] = mn;
#pragma unroll
      for (int p = 0; p < NP; ++p) {
        float e[4];
#pragma unroll
        for (int r = 0; r < 4; ++r) { e[r] = __builtin_amdgcn_exp2f(fmaf(sc[p][t][r], C2, -ms)); ls += e[r]; }
        pf[p >> 1][t][(p & 1) * 2] = pk_bf16(e[0], e[1]);
        pf[p >> 1][t][(p & 1) * 2 + 1] = pk_bf16(e[2], e[3]);
      }
      l[t] = ls;
    }
    STAMP(6);
    __builtin_amdgcn_sched_barrier(0);
#pragma unroll
    for (int pp = 0; pp < NP / 2; ++pp) {
#pragma unroll
      for (int e = 0; e < 4; ++e) {
        u32x4 lo, hi;
#pragma unroll
        for (int h = 0; h < 2; ++h) {
          const u32x4* v = vf[2 * pp + h];
          lo[2 * h] = __builtin_amdgcn_perm(v[1][e], v[0][e], 0x05040100u);
          lo[2 * h + 1] = __builtin_amdgcn_perm(v[3][e], v[2][e], 0x05040100u);
          hi[2 * h] = __builtin_amdgcn_perm(v[1][e], v[0][e], 0x07060302u);
          hi[2 * h + 1] = __builtin_amdgcn_perm(v[3][e], v[2][e], 0x07060302u);
        }
#pragma unroll
        for (int t = 0; t < 2; ++t) {
          acc[t][2 * e] = mfma(lo, pf[pp][t], acc[t][2 * e]);
          acc[t][2 * e + 1] = mfma(hi, pf[pp][t], acc[t][2 * e + 1]);
        }
      }
      __builtin_amdgcn_sched_barrier(0);
    }
  };
  round(std::integral_constant<int, 0>{});
  if (u1 - u0 > NW * NP) round(std::integral_constant<int, 1>{});
  STAMP(7);
#pragma unroll
  for (int t = 0; t < 2; ++t) {
    const float ls = xg_sum(l[t]);
    if (g == 0) *reinterpret_cast<f32x2*>(lds + L_ML + (wave * 32 + 16 * t + c) * 8) = f32x2{M[t], ls};
    if ((unsigned)(4 * t + (c & 3) - blo) < (unsigned)nseq) {
#pragma unroll
      for (int d = 0; d < 8; ++d) *reinterpret_cast<f32x4*>(lds + wreg + (16 * t + c) * OROW + g * 128 + d * 16) = acc[t][d];
    }
  }
#pragma nounroll
  for (unsigned it = 0; it < SPIN; ++it) {
    const bool ok = (flo == OOB) | ((fl[0] == (unsigned)tag) & (fl[1] == (unsigned)(tag >> 32)));
    if (__all(ok)) break;
    asm volatile("s_sleep 2" ::: "memory");
    fl = __builtin_amdgcn_raw_buffer_load_b64(wrs, flo, 0, 16);
  }
  STAMP(8);
  __syncthreads();
  STAMP(9);
  {
    const int x = tid & 31;
    for (int k = tid >> 5; k < 4 * nseq; k += NW * 2) {
      const int b = blo + (k >> 2), row = (b >> 2) * 16 + (k & 3) * 4 + (b & 3);
      f32x2 ml[NW];
      f32x4 ow[NW];
#pragma unroll
      for (int w = 0; w < NW; ++w) {
        ml[w] = *reinterpret_cast<const f32x2*>(lds + L_ML + (w * 32 + row) * 8);
        ow[w] = *reinterpret_cast<const f32x4*>(lds + w * WREG + row * OROW + x * 16);
      }
      float Mx = ml[0][0];
#pragma unroll
      for (int w = 1; w < NW; ++w) Mx = fmaxf(Mx, ml[w][0]);
      const float ms = (Mx == -INFINITY) ? 0.f : Mx * C2;
      float L = 0.f;
      f32x4 o = {0.f, 0.f, 0.f, 0.f};
#pragma unroll
      for (int w = 0; w < NW; ++w) {
        const float f = __builtin_amdgcn_exp2f(fmaf(ml[w][0], C2, -ms));
        L = fmaf(f, ml[w][1], L);
        o += f * ow[w];
      }
      const unsigned slot = (unsigned)(kvh * 32 + row) * WGH + wi;
      const unsigned e0 = (__float_as_uint(o[0]) & ~1u) | ((__float_as_uint(Mx) >> x) & 1u);
      const unsigned e1 = (__float_as_uint(o[1]) & ~1u) | ((__float_as_uint(L) >> x) & 1u);
      __builtin_amdgcn_raw_buffer_store_b128(u32x4{canon(e0), canon(e1), canon(__float_as_uint(o[2])), canon(__float_as_uint(o[3]))}, wrs,
                                             W_O + slot * (HD * 4) + x * 16, 0, 16);
    }
  }
  STAMP(10);
  if (wave == NW - 1) {
    if (wi == 0) __builtin_amdgcn_raw_buffer_store_b128(tag4, wrs, lane < 8 ? lane * 16 : OOB, W_EPOCH + kvh * 128, 16);
    constexpr int NL = 6;
    const int hw = lane >> 5, x = lane & 31;
    float Mx = -INFINITY, L = 0.f;
    f32x4 o = {0.f, 0.f, 0.f, 0.f};
    unsigned m = pmask;
    while (m) {
      unsigned vo[NL];
#pragma unroll
      for (int k = 0; k < NL; ++k) {
        const unsigned s0 = m ? (rowslot + __builtin_ctz(m)) * (HD * 4) + W_O : OOB;
        m &= m - 1;
        const unsigned s1 = m ? (rowslot + __builtin_ctz(m)) * (HD * 4) + W_O : OOB;
        m &= m - 1;
        vo[k] = (hw ? s1 : s0) + x * 16;
      }
      u32x4 ov[NL];
      unsigned po[NL];
#pragma unroll
      for (int k = 0; k < NL; ++k) { po[k] = vo[k]; ov[k] = u32x4{0u, 0u, 0u, 0u}; }
#pragma nounroll
      for (unsigned it = 0; it < SPIN; ++it) {
        u32x4 nv[NL];
#pragma unroll
        for (int k = 0; k < NL; ++k) nv[k] = __builtin_amdgcn_raw_buffer_load_b128(wrs, po[k], 0, 16);
        bool ok = true;
#pragma unroll
        for (int k = 0; k < NL; ++k) {
          const bool got = (nv[k][0] != SENT) & (nv[k][1] != SENT) & (nv[k][2] != SENT) & (nv[k][3] != SENT);
          ov[k] = po[k] >= OOB ? ov[k] : nv[k];
          po[k] = got ? OOB : po[k];
          ok &= got;
        }
        if (__all(ok)) break;
        asm volatile("s_sleep 1" ::: "memory");
      }
      float mk[NL], lk[NL], mn = Mx;
#pragma unroll
      for (int k = 0; k < NL; ++k) {
        const u64 bm = __ballot(ov[k][0] & 1), bl = __ballot(ov[k][1] & 1);
        mk[k] = vo[k] >= OOB ? -INFINITY : __uint_as_float(hw ? (unsigned)(bm >> 32) : (unsigned)bm);
        lk[k] = __uint_as_float(hw ? (unsigned)(bl >> 32) : (unsigned)bl);
        mn = fmaxf(mn, mk[k]);
      }
      {
        auto s = __builtin_amdgcn_permlane32_swap(__float_as_uint(mn), __float_as_uint(mn), false, false);
        mn = fmaxf(__uint_as_float(s[0]), __uint_as_float(s[1]));
      }
      const float ms = (mn == -INFINITY) ? 0.f : mn * C2;
      const float alpha = __builtin_amdgcn_exp2f(Mx * C2 - ms);
      Mx = mn;
      L *= alpha;
      o *= alpha;
#pragma unroll
      for (int k = 0; k < NL; ++k) {
        const float w = __builtin_amdgcn_exp2f(fmaf(mk[k], C2, -ms));
        L = fmaf(w, lk[k], L);
        o += w * __builtin_bit_cast(f32x4, ov[k]);
      }
    }
    {
      auto s = __builtin_amdgcn_permlane32_swap(__float_as_uint(L), __float_as_uint(L), false, false);
      L = __uint_as_float(s[0]) + __uint_as_float(s[1]);
    }
#pragma unroll
    for (int r = 0; r < 4; ++r) {
      auto s = __builtin_amdgcn_permlane32_swap(__float_as_uint(o[r]), __float_as_uint(o[r]), false, false);
      o[r] = __uint_as_float(s[0]) + __uint_as_float(s[1]);
    }
    const int d0 = 32 * (x >> 3) + (x & 7);
    const int lb = clen;
    if (lb < LK && !(Mx * SCALE >= MASKV + 256.f)) {
      const float Mn = Mx * SCALE, Mt = fmaxf(Mn, MASKV);
      const float wv = (L > 0.f) ? __expf(Mn - Mt) : 0.f, wm = __expf(MASKV - Mt);
      f32x4 sv = {0.f, 0.f, 0.f, 0.f};
      for (int pos = lb; pos < LK; ++pos) {
        const int phys = *reinterpret_cast<const int*>(lds + L_BT + (cb * NPG + (pos >> 4)) * 4);
        const unsigned short* vp = vc + ((size_t)phys * PS + (pos & 15)) * (NKV * HD) + kvh * HD + d0;
#pragma unroll
        for (int r = 0; r < 4; ++r) sv[r] += __uint_as_float((unsigned)vp[8 * r] << 16);
      }
      o = o * wv + sv * wm;
      L = L * wv + wm * (float)(LK - lb);
    }
    const float inv = 1.f / L;
    unsigned short* op = out + (cb * NH + kvh * 4 + ch) * HD + d0 + 16 * hw;
    op[0] = __builtin_bit_cast(unsigned short, (__bf16)((hw ? o[2] : o[0]) * inv));
    op[8] = __builtin_bit_cast(unsigned short, (__bf16)((hw ? o[3] : o[1]) * inv));
  }
  asm volatile("s_waitcnt vmcnt(0)" ::: "memory");
  STAMP(11);
  STAMP_FLUSH(ws, blockIdx.x * NW + wave);
}

__global__ __launch_bounds__(256) void diag_k(const float* ws) {
  __shared__ unsigned long long sm[2][256];
  const unsigned long long* p = (const unsigned long long*)((const char*)ws + ST_OFF);
  unsigned long long mn = ~0ull, mx = DIAG_MIN ? ~0ull : 0;
  for (int w = threadIdx.x; w < 2048; w += 256) mn = min(mn, p[(size_t)w * NST]);
  for (int w = threadIdx.x; w < 2048; w += 256) {
    if (((w >> 3) & 7) != DIAG_HEAD) continue;
    unsigned long long t = p[(size_t)w * NST + DIAG_I]; mx = DIAG_MIN ? min(mx, t) : max(mx, t); }
  sm[0][threadIdx.x] = mn; sm[1][threadIdx.x] = mx;
  __syncthreads();
  if (threadIdx.x == 0) {
    for (int i = 0; i < 256; ++i) { mn = min(mn, sm[0][i]); mx = DIAG_MIN ? min(mx, sm[1][i]) : max(mx, sm[1][i]); }
    unsigned long long q = (mx - mn + 5) / 10;
    if (q > 400) q = 400;
    const unsigned long long target = mn + 10000 + 800 * q;
    for (int it = 0; it < 1000000; ++it) { if (__builtin_amdgcn_s_memrealtime() >= target) break; __builtin_amdgcn_s_sleep(1); }
  }
}
}

extern "C" void kernel_launch(void* const* d_in, const int* in_sizes, int n_in, void* d_out, int out_size, void* d_ws, size_t ws_size,
                              hipStream_t stream) {
  attn_kernel<<<NKV * WGH, NW * 64, 0, stream>>>((const unsigned short*)d_in[0], (const unsigned short*)d_in[1], (const unsigned short*)d_in[2],
                                                 (const int*)d_in[3], (const int*)d_in[4], (unsigned short*)d_out, (unsigned char*)d_ws,
                                                 (unsigned)in_sizes[1] * 2u);
  diag_k<<<1, 256, 0, stream>>>((const float*)d_ws);
}
```

```cpp
#include <hip/hip_runtime.h>
#include <hip/hip_bf16.h>
#include <stdint.h>
#include <type_traits>

typedef __attribute__((ext_vector_type(8))) __bf16 bf16x8;
typedef __attribute__((ext_vector_type(4))) float f32x4;
typedef __attribute__((ext_vector_type(2))) float f32x2;
typedef __attribute__((ext_vector_type(4))) unsigned u32x4;
typedef __attribute__((ext_vector_type(2))) unsigned u32x2;
typedef unsigned long long u64;

#define STAMP(i)
#define STAMP_DECL
#define STAMP_FLUSH(ws, slot)

namespace {
constexpr int NB = 8, NH = 32, NKV = 8, HD = 128, NPG = 256, PS = 16, LK = NPG * PS;
constexpr int WGH = 32;
constexpr int NW = 8;
constexpr int NP = 4;
constexpr float SCALE = 0.08838834764831845f;
constexpr float C2 = SCALE * 1.4426950408889634f;
constexpr float LN2 = 0.6931471805599453f;
constexpr float MASKV = -998244352.0f;
constexpr unsigned TOKB = NKV * HD * 2, PAGEB = PS * TOKB;
constexpr unsigned OOB = 0x80000000u;
constexpr unsigned TMASK = 0xFFFFFEFEu;
constexpr unsigned SPIN = 1u << 18;
#define FV 2
#define FAUX 0
constexpr unsigned CODE_PF = 17 * 1024;

constexpr int PIECE = 1040, KPAGE = 4 * PIECE, OROW = 528, WREG = 32 * OROW;
constexpr int L_Q = NW * WREG, L_BT = L_Q + NB * PIECE, L_M = L_BT + NB * NPG * 4, L_L = L_M + 32 * NW * 4, L_FIRST = L_L + 32 * NW * 4, L_CNT = L_FIRST + 32, L_LIST = L_CNT + 16, L_END = L_LIST + 32 * 4;
static_assert(NP * KPAGE <= WREG && L_END <= 160 * 1024, "lds");

constexpr unsigned W_EPOCH = 0;
constexpr unsigned W_FLAG = NKV * 128;
constexpr unsigned W_O = 65536;
constexpr unsigned W_END = W_O + NKV * 32 * WGH * HD * 4;

__device__ __forceinline__ float max3f(float a, float b, float c) {
  float r;
  asm("v_max3_f32 %0, %1, %2, %3" : "=v"(r) : "v"(a), "v"(b), "v"(c));
  return r;
}
__device__ __forceinline__ float xg_max(float x) {
  auto s = __builtin_amdgcn_permlane16_swap(__float_as_uint(x), __float_as_uint(x), false, false);
  x = max3f(__uint_as_float(s[0]), __uint_as_float(s[1]), __uint_as_float(s[1]));
  auto t = __builtin_amdgcn_permlane32_swap(__float_as_uint(x), __float_as_uint(x), false, false);
  return max3f(__uint_as_float(t[0]), __uint_as_float(t[1]), __uint_as_float(t[1]));
}
__device__ __forceinline__ float xg_sum(float x) {
  auto s = __builtin_amdgcn_permlane16_swap(__float_as_uint(x), __float_as_uint(x), false, false);
  x = __uint_as_float(s[0]) + __uint_as_float(s[1]);
  auto t = __builtin_amdgcn_permlane32_swap(__float_as_uint(x), __float_as_uint(x), false, false);
  return __uint_as_float(t[0]) + __uint_as_float(t[1]);
}
template <int CTRL> __device__ __forceinline__ float dpp(float x) {
  return __builtin_bit_cast(float, __builtin_amdgcn_mov_dpp(__builtin_bit_cast(int, x), CTRL, 0xf, 0xf, true));
}
template <int CTRL> __device__ __forceinline__ int dppi(int x) { return __builtin_amdgcn_mov_dpp(x, CTRL, 0xf, 0xf, true); }
__device__ __forceinline__ int min8(int x) { x = min(x, dppi<0xB1>(x)); x = min(x, dppi<0x4E>(x)); return min(x, dppi<0x141>(x)); }
__device__ __forceinline__ int max8(int x) { x = max(x, dppi<0xB1>(x)); x = max(x, dppi<0x4E>(x)); return max(x, dppi<0x141>(x)); }
__device__ __forceinline__ unsigned min8u(unsigned x) {
  x = min(x, (unsigned)dppi<0xB1>((int)x)); x = min(x, (unsigned)dppi<0x4E>((int)x)); return min(x, (unsigned)dppi<0x141>((int)x));
}
__device__ __forceinline__ float row_max(float x) {
  x = fmaxf(x, dpp<0xB1>(x)); x = fmaxf(x, dpp<0x4E>(x)); x = fmaxf(x, dpp<0x141>(x)); return fmaxf(x, dpp<0x128>(x));
}
__device__ __forceinline__ float row_sum(float x) {
  x += dpp<0xB1>(x); x += dpp<0x4E>(x); x += dpp<0x141>(x); return x + dpp<0x128>(x);
}
__device__ __forceinline__ float max8f(f32x4 a, f32x4 b) { return max3f(max3f(max3f(a[0], a[1], a[2]), a[3], b[0]), max3f(b[1], b[2], b[3]), a[0]); }
__device__ __forceinline__ float rel(float m, float ref) { return __builtin_amdgcn_exp2f(m - ((ref == -INFINITY) ? 0.f : ref)); }
__device__ __forceinline__ unsigned pk_bf16(float lo, float hi) {
  typedef __attribute__((ext_vector_type(2))) __bf16 bf16x2;
  bf16x2 v = {(__bf16)lo, (__bf16)hi};
  return __builtin_bit_cast(unsigned, v);
}
__device__ __forceinline__ f32x4 mfma(u32x4 a, u32x4 b, f32x4 c) {
  return __builtin_amdgcn_mfma_f32_16x16x32_bf16(__builtin_bit_cast(bf16x8, a), __builtin_bit_cast(bf16x8, b), c, 0, 0, 0);
}
__device__ __forceinline__ unsigned trailer(u32x4 v) {
  return __builtin_amdgcn_perm(__builtin_amdgcn_perm(v[3], v[2], 0x0c0c0400u), __builtin_amdgcn_perm(v[1], v[0], 0x0c0c0400u), 0x05040100u);
}
template <int N> __device__ __forceinline__ void wait_vm() { asm volatile("s_waitcnt vmcnt(%0)" ::"n"(N) : "memory"); }
template <int LO, int SO> __device__ __forceinline__ void dma_nt(unsigned lds, unsigned voff, __amdgpu_buffer_rsrc_t rs, unsigned soff) {
  unsigned t;
  asm volatile("s_add_u32 m0, %1, %5\n\ts_add_u32 %0, %4, %6\n\tbuffer_load_dwordx4 %2, %3, %0 offen nt lds"
               : "=&s"(t) : "s"(lds), "v"(voff), "s"(rs), "s"(soff), "n"(LO), "n"(SO) : "memory", "scc");
}
template <int P> __device__ __forceinline__ void dma_page(unsigned lds, unsigned voff, __amdgpu_buffer_rsrc_t rs, unsigned soff) {
  dma_nt<P * KPAGE, 0>(lds, voff, rs, soff);
  dma_nt<P * KPAGE + PIECE, 4 * TOKB>(lds, voff, rs, soff);
  dma_nt<P * KPAGE + 2 * PIECE, 8 * TOKB>(lds, voff, rs, soff);
  dma_nt<P * KPAGE + 3 * PIECE, 12 * TOKB>(lds, voff, rs, soff);
}
__device__ __forceinline__ void dma(unsigned lds, unsigned voff, __amdgpu_buffer_rsrc_t rs, unsigned soff) {
  asm volatile("s_mov_b32 m0, %0\n\ts_nop 0\n\tbuffer_load_dwordx4 %1, %2, %3 offen lds" ::"s"(lds), "v"(voff), "s"(rs), "s"(soff) : "memory");
}

__global__ __launch_bounds__(NW * 64, 2) void attn_kernel(const unsigned short* __restrict__ q, const unsigned short* __restrict__ kc,
                                                          const unsigned short* __restrict__ vc, const int* __restrict__ bt,
                                                          const int* __restrict__ lens, unsigned short* __restrict__ out,
                                                          unsigned char* __restrict__ ws, unsigned kv_bytes) {
  __shared__ __attribute__((aligned(16))) unsigned char lds[L_END];
  STAMP_DECL;
  STAMP(0);
  unsigned pf_dummy;
  u64 pc;
  {
    asm volatile("s_getpc_b64 %1\n\tglobal_load_dword %0, %2, %1" : "=&v"(pf_dummy), "=&s"(pc) : "v"(min(threadIdx.x * 128u, CODE_PF - 128u)));
    __builtin_amdgcn_sched_barrier(0);
  }
  const int tid = threadIdx.x, lane = tid & 63, wave = __builtin_amdgcn_readfirstlane(tid >> 6);
  const int g = lane >> 4, c = lane & 15;
  const int kvh = blockIdx.x & (NKV - 1), wi = blockIdx.x >> 3;
  const unsigned ldsb = (unsigned)(size_t)lds;
  asm volatile("" ::"s"(q), "s"(kc), "s"(vc), "s"(bt), "s"(lens), "s"(out), "s"(ws), "s"(kv_bytes));

  const int sb = lane & 7;
  const u32x4 tv = *reinterpret_cast<const u32x4*>(bt + wave * NPG + 4 * lane);
  const u32x4 t0 = *reinterpret_cast<const u32x4*>(bt + 4 * lane);
  int len = lens[sb];
  u64 tag = *(const __attribute__((address_space(4))) u64*)(ws + W_EPOCH + kvh * 128);
  __builtin_amdgcn_sched_barrier(0);
  const unsigned swz = (unsigned)(c ^ ((g & 2) << 2)) * 16;
  {
    const __amdgpu_buffer_rsrc_t qrs = __builtin_amdgcn_make_buffer_rsrc((void*)q, 0, NB * NH * HD * 2, 0x00020000);
    dma(ldsb + L_Q + wave * PIECE, g * 256 + swz, qrs, (wave * NH + kvh * 4) * HD * 2);
  }
  const __amdgpu_buffer_rsrc_t krs = __builtin_amdgcn_make_buffer_rsrc((void*)(kc + kvh * HD), 0, kv_bytes - kvh * HD * 2, 0x00020000);
  const __amdgpu_buffer_rsrc_t vrs = __builtin_amdgcn_make_buffer_rsrc((void*)(vc + kvh * HD), 0, kv_bytes - kvh * HD * 2, 0x00020000);
  const __amdgpu_buffer_rsrc_t wrs = __builtin_amdgcn_make_buffer_rsrc((void*)ws, 0, W_END, 0x00020000);
  if (tid == 0) *reinterpret_cast<int*>(lds + L_CNT) = 0;
  unsigned voff = g * TOKB + c * 16, koff = g * TOKB + swz;
  const unsigned wreg = wave * WREG;
  unsigned krd = wreg + (c & 3) * PIECE + (c >> 2) * 256 + ((4 * g) ^ (c & 8)) * 16;
  unsigned qrd = L_Q + (c >> 2) * 256 + ((4 * g) ^ (c & 8)) * 16;
  f32x4 acc[2][8];
#pragma unroll
  for (int t = 0; t < 2; ++t)
#pragma unroll
    for (int d = 0; d < 8; ++d) acc[t][d] = f32x4{0.f, 0.f, 0.f, 0.f};
  float M[2] = {-INFINITY, -INFINITY}, l[2] = {0.f, 0.f};
  unsigned kdst = ldsb + wreg;
  asm volatile("" : "+v"(voff), "+v"(koff), "+v"(krd), "+v"(qrd), "+s"(kdst) : "s"(krs), "s"(vrs), "s"(wrs));
  __builtin_amdgcn_sched_barrier(0);
  len = min(max(len, 0), LK);
  const int np = (len + 15) >> 4;
  int psn = np;
  { const int t = dppi<0x111>(psn); psn += sb >= 1 ? t : 0; }
  { const int t = dppi<0x112>(psn); psn += sb >= 2 ? t : 0; }
  { const int t = dppi<0x114>(psn); psn += sb >= 4 ? t : 0; }
  int ps8 = __builtin_amdgcn_readlane(psn, 7);
  {
    int first = min8(len >> 4);
#pragma unroll
    for (int k = 0; k < 4; ++k) {
      const u64 m = __ballot(tv[k] != t0[k]);
      first = min(first, m ? 4 * (int)__builtin_ctzll(m) + k : NPG);
    }
    *reinterpret_cast<u32x4*>(lds + L_BT + wave * (NPG * 4) + lane * 16) = tv;
    if (lane == 0) *reinterpret_cast<int*>(lds + L_FIRST + wave * 4) = first;
  }
  int uo = wave + (lane & 56), bta = L_BT + sb * (NPG * 4);
  asm volatile("" : "+v"(psn), "+s"(ps8), "+v"(uo), "+v"(bta), "+s"(tag));
  tag += 1;
  STAMP(1);
  asm volatile("s_waitcnt vmcnt(%0) lgkmcnt(0)\n\ts_barrier" ::"n"(0) : "memory");
  asm volatile("" ::"v"(pf_dummy));
  STAMP(2);
  int Sv, bnd;
  {
    const u32x4 a = *reinterpret_cast<const u32x4*>(lds + L_FIRST), b = *reinterpret_cast<const u32x4*>(lds + L_FIRST + 16);
    Sv = (int)min(min(min(a[0], a[1]), min(a[2], a[3])), min(min(b[0], b[1]), min(b[2], b[3])));
    Sv = Sv < 8 ? 0 : Sv;
    asm volatile("" : "+v"(Sv));
    const int P = ps8 - NB * Sv;
    const float fS = (float)Sv, fP = (float)P;
    const float fn = 288.f * fS * __builtin_amdgcn_rcpf(fmaf(9.f, fS, fmaf(8.f, fP, 1e-3f)));
    const int nsh = min(max((int)(fn + 0.5f), (Sv + 63) >> 6), WGH - ((P + 63) >> 6));
    const bool lo = lane <= nsh;
    const int cnt = lo ? nsh : WGH - nsh, tot = lo ? Sv : P, i = lo ? lane : lane - nsh;
    const int qq = (int)(((lo ? fS : fP) + 0.5f) * __builtin_amdgcn_rcpf((float)max(cnt, 1)));
    bnd = (lo ? 0 : Sv) + i * qq + min(i, tot - qq * cnt);
  }
  const int u0 = __builtin_amdgcn_readlane(bnd, wi), u1 = __builtin_amdgcn_readlane(bnd, wi + 1);
  const int S = __builtin_amdgcn_readfirstlane(Sv);
  const int offn = psn - sb * Sv, offb = offn - np + Sv;
  unsigned so_v;
  int info_v;
  {
    const int u = u0 + uo;
    const bool sh = u < S;
    const bool hit = (u < u1) & (sh ? sb == 0 : (u >= offb) & (u < offn));
    const int j = sh ? u : min(max(S + u - offb, 0), NPG - 1);
    int phys = *reinterpret_cast<const int*>(lds + bta + j * 4);
    asm volatile("" : "+v"(phys));
    so_v = (unsigned)min8u(hit ? (unsigned)phys * PAGEB : OOB);
    info_v = hit ? (sh ? 16 | 8 << 8 : min(16, len - 16 * j) | sb << 8) : 0;
  }
  const int cb = (wi >> 4) * 4 + (wi & 3), ch = (wi & 15) >> 2;

  const unsigned tl = (unsigned)tag;
  const unsigned tag32 = ((tl << 1) & 0xFEu) | (((tl + (tl >> 7)) << 9) & 0xFE00u) | (((tl + (tl >> 14)) << 16) & 0xFF0000u) | ((tl + (tl >> 22)) << 24);
  const u32x4 tag4 = {(unsigned)tag, (unsigned)(tag >> 32), (unsigned)tag, (unsigned)(tag >> 32)};
  int blo, nseq, clen;
  unsigned pmask;
  unsigned so[NP], f2 = 0, pro[4];
  const unsigned char* tp = ws;

  auto issue_k = [&](int rd) __attribute__((always_inline)) {
#pragma unroll
    for (int p = 0; p < NP; ++p) so[p] = __builtin_amdgcn_readlane(so_v, 8 * (NP * rd + p));
    STAMP(3);
    __builtin_amdgcn_s_setprio(1);
    dma_page<0>(kdst, koff, krs, so[0]);
    dma_page<1>(kdst, koff, krs, so[1]);
    dma_page<2>(kdst, koff, krs, so[2]);
    dma_page<3>(kdst, koff, krs, so[3]);
    __builtin_amdgcn_s_setprio(0);
    __builtin_amdgcn_sched_barrier(0);
    STAMP(4);
  };
  auto wg_max = [&](int lr) __attribute__((always_inline)) {
    const f32x4 ma = *reinterpret_cast<const f32x4*>(lds + L_M + lr * NW * 4), mb = *reinterpret_cast<const f32x4*>(lds + L_M + lr * NW * 4 + 16);
    return max8f(ma, mb);
  };
  bool have = false;

  auto round = [&](auto ntc, auto multic, int rd, bool last) __attribute__((always_inline)) {
    constexpr int NT = decltype(ntc)::value;
    constexpr bool MULTI = decltype(multic)::value;
    int info[NP];
    {
      const int inf = max8(info_v);
#pragma unroll
      for (int p = 0; p < NP; ++p) info[p] = __builtin_amdgcn_readlane(inf, 8 * (NP * rd + p));
    }
    u32x4 qf[NT][4];
    int csq[NT];
#pragma unroll
    for (int t = 0; t < NT; ++t) {
      csq[t] = (blo + 4 * t + (c & 3)) & 7;
#pragma unroll
      for (int s = 0; s < 4; ++s) qf[t][s] = *reinterpret_cast<const u32x4*>(lds + qrd + csq[t] * PIECE + s * 16);
    }
    f32x4 sc[NP][NT];
    float mx[NT];
#pragma unroll
    for (int t = 0; t < NT; ++t) mx[t] = -INFINITY;
#pragma unroll
    for (int p = 0; p < NP; ++p) {
      if (p == 0) wait_vm<12>();
      if (p == 1) wait_vm<8>();
      if (p == 2) wait_vm<4>();
      if (p == 3) wait_vm<0>();
      u32x4 kf[4];
#pragma unroll
      for (int s = 0; s < 4; ++s) kf[s] = *reinterpret_cast<const u32x4*>(lds + krd + p * KPAGE + s * 16);
      const int nv = info[p] & 255, sq = info[p] >> 8;
#pragma unroll
      for (int t = 0; t < NT; ++t) {
        f32x4 a = {0.f, 0.f, 0.f, 0.f};
#pragma unroll
        for (int s = 0; s < 4; ++s) a = mfma(kf[s], qf[t][s], a);
        const int lim = ((sq == 8) | (sq == csq[t])) ? nv - g : 0;
#pragma unroll
        for (int r = 0; r < 4; ++r) a[r] = 4 * r < lim ? a[r] : -INFINITY;
        mx[t] = max3f(max3f(mx[t], a[0], a[1]), a[2], a[3]);
        asm volatile("" : "+v"(a), "+v"(mx[t]));
        sc[p][t] = a;
      }
    }
    __builtin_amdgcn_sched_barrier(0);
    u32x4 vf[NP][4];
#pragma unroll
    for (int p = 0; p < NP; ++p)
#pragma unroll
      for (int i = 0; i < 4; ++i) vf[p][i] = __builtin_amdgcn_raw_buffer_load_b128(vrs, voff, so[p] + i * 4 * TOKB, 2);
    if (__builtin_expect(wave == 0, 0)) __builtin_amdgcn_raw_buffer_store_b128(tag4, wrs, lane < 8 ? lane * 16 : OOB, W_FLAG + (kvh * 32 + wi) * 128, 16);
    if (wave < 2) f2 = __builtin_amdgcn_raw_buffer_load_b32(wrs, pro[0], 0, FAUX);
    STAMP(5);
    __builtin_amdgcn_sched_barrier(0);
#pragma unroll
    for (int t = 0; t < NT; ++t)
      mx[t] = fmaxf(M[t], __builtin_ceilf(xg_max(mx[t]) * C2));
    if (last) {
#pragma unroll
      for (int t = 0; t < NT; ++t)
        if (g == 0) *reinterpret_cast<float*>(lds + L_M + ((16 * t + c) * NW + wave) * 4) = mx[t];
      if (lane == 0) asm volatile("ds_add_u32 %0, %1" ::"v"(ldsb + L_CNT), "v"(1) : "memory");
    }
    const u32x4 ones = {0x3F803F80u, 0x3F803F80u, 0x3F803F80u, 0x3F803F80u};
    u32x4 pf[NT];
    f32x4 ls[NT];
#pragma unroll
    for (int t = 0; t < NT; ++t) {
      const float ms = (mx[t] == -INFINITY) ? 0.f : mx[t];
#pragma unroll
      for (int p = 0; p < NP; ++p)
#pragma unroll
        for (int r = 0; r < 4; ++r) sc[p][t][r] = __builtin_amdgcn_exp2f(fmaf(sc[p][t][r], C2, -ms));
      ls[t] = f32x4{0.f, 0.f, 0.f, 0.f};
      if constexpr (MULTI) {
        const float alpha = rel(M[t], mx[t]);
        ls[t][0] = l[t] * alpha;
        if (rd != 0) {
#pragma unroll
          for (int d = 0; d < 8; ++d) acc[t][d] *= alpha;
        }
      }
      M[t] = mx[t];
      pf[t] = u32x4{pk_bf16(sc[0][t][0], sc[0][t][1]), pk_bf16(sc[0][t][2], sc[0][t][3]), pk_bf16(sc[1][t][0], sc[1][t][1]), pk_bf16(sc[1][t][2], sc[1][t][3])};
      ls[t] = mfma(ones, pf[t], ls[t]);
      asm volatile("" : "+v"(sc[2][t]), "+v"(sc[3][t]), "+v"(pf[t]));
    }
    STAMP(6);
    __builtin_amdgcn_sched_barrier(0);
    auto pv = [&](int pp) __attribute__((always_inline)) {
#pragma unroll
      for (int e = 0; e < 4; ++e) {
        u32x4 lo, hi;
#pragma unroll
        for (int h = 0; h < 2; ++h) {
          const u32x4* v = vf[2 * pp + h];
          lo[2 * h] = __builtin_amdgcn_perm(v[1][e], v[0][e], 0x05040100u);
          lo[2 * h + 1] = __builtin_amdgcn_perm(v[3][e], v[2][e], 0x05040100u);
          hi[2 * h] = __builtin_amdgcn_perm(v[1][e], v[0][e], 0x07060302u);
          hi[2 * h + 1] = __builtin_amdgcn_perm(v[3][e], v[2][e], 0x07060302u);
        }
#pragma unroll
        for (int t = 0; t < NT; ++t) {
          acc[t][2 * e] = mfma(lo, pf[t], acc[t][2 * e]);
          acc[t][2 * e + 1] = mfma(hi, pf[t], acc[t][2 * e + 1]);
        }
      }
    };
    wait_vm<8>();
    const int cnt = *reinterpret_cast<const int*>(lds + L_CNT);
    asm volatile("" ::: "memory");
    f32x4 ma[NT], mb[NT];
#pragma unroll
    for (int t = 0; t < NT; ++t) {
      ma[t] = *reinterpret_cast<const f32x4*>(lds + L_M + (16 * t + c) * NW * 4);
      mb[t] = *reinterpret_cast<const f32x4*>(lds + L_M + (16 * t + c) * NW * 4 + 16);
    }
    __builtin_amdgcn_sched_barrier(0);
    pv(0);
    __builtin_amdgcn_sched_barrier(0);
    have = last && __builtin_amdgcn_readfirstlane(cnt) == NW;
#pragma unroll
    for (int t = 0; t < NT; ++t) {
      const float fk = have ? rel(mx[t], max8f(ma[t], mb[t])) : 1.f;
      pf[t] = u32x4{pk_bf16(sc[2][t][0] * fk, sc[2][t][1] * fk), pk_bf16(sc[2][t][2] * fk, sc[2][t][3] * fk),
                    pk_bf16(sc[3][t][0] * fk, sc[3][t][1] * fk), pk_bf16(sc[3][t][2] * fk, sc[3][t][3] * fk)};
      ls[t][0] *= fk;
      l[t] = mfma(ones, pf[t], ls[t])[0];
      if (last && g == 0) *reinterpret_cast<float*>(lds + L_L + ((16 * t + c) * NW + wave) * 4) = l[t];
#pragma unroll
      for (int d = 0; d < 8; ++d) acc[t][d] *= fk;
#pragma unroll
      for (int d = 0; d < 8; ++d) asm volatile("" : "+v"(acc[t][d]));
    }
    __builtin_amdgcn_sched_barrier(0);
    pv(1);
    __builtin_amdgcn_sched_barrier(0);
  };
  auto store_o = [&](auto ntc) __attribute__((always_inline)) {
    constexpr int NT = decltype(ntc)::value;
    STAMP(7);
    if (__builtin_expect(!have, 0)) {
#pragma nounroll
      for (unsigned it = 0; it < 16 * SPIN && __hip_atomic_load(reinterpret_cast<int*>(lds + L_CNT), __ATOMIC_RELAXED, __HIP_MEMORY_SCOPE_WORKGROUP) < NW; ++it) __builtin_amdgcn_s_sleep(1);
      asm volatile("" ::: "memory");
#pragma unroll
      for (int t = 0; t < NT; ++t) {
        const float f = rel(M[t], wg_max(16 * t + c));
        if (g == 0) *reinterpret_cast<float*>(lds + L_L + ((16 * t + c) * NW + wave) * 4) = l[t] * f;
#pragma unroll
        for (int d = 0; d < 8; ++d) acc[t][d] *= f;
      }
    }
#pragma unroll
    for (int t = 0; t < NT; ++t)
      if (4 * t + (c & 3) < nseq) {
#pragma unroll
        for (int d = 0; d < 8; ++d) *reinterpret_cast<f32x4*>(lds + wreg + (16 * t + c) * OROW + g * 128 + d * 16) = acc[t][d];
      }
  };
  using I1 = std::integral_constant<int, 1>;
  using I2 = std::integral_constant<int, 2>;
  issue_k(0);
  {
    const unsigned sm = (unsigned)__ballot((u1 > u0) & ((u0 < S) | ((u0 < offn) & (u1 > offb)))) & 255u;
    blo = sm ? __builtin_ctz(sm) : 0;
    nseq = __builtin_popcount(sm);
    clen = __builtin_amdgcn_readlane(len, cb);
    const int cofl = __builtin_amdgcn_readlane(offb, cb), cofh = __builtin_amdgcn_readlane(offn, cb);
    const int a0 = bnd, a1 = __builtin_amdgcn_update_dpp(0, bnd, 0x130, 0xf, 0xf, false);
    pmask = (unsigned)__ballot((a1 > a0) & ((a0 < S) | ((a0 < cofh) & (a1 > cofl))));
    asm volatile("" : "+s"(blo), "+s"(nseq), "+s"(clen), "+s"(pmask));
  }
  constexpr int NL = 6;
  const int x = tid & 31, hw = lane >> 5, nprod = __builtin_popcount(pmask);
  unsigned vo[NL];
  auto batch = [&](int r0) __attribute__((always_inline)) {
#pragma unroll
    for (int k = 0; k < NL; ++k) {
      const int r = r0 + 2 * k + hw;
      unsigned s = *reinterpret_cast<const unsigned*>(lds + L_LIST + min(r, 31) * 4);
      asm volatile("" : "+v"(s));
      vo[k] = r < nprod ? s + x * 16 : OOB;
    }
  };
  if (wave == NW - 1) {
    if (lane < 32 && ((pmask >> lane) & 1))
      *reinterpret_cast<unsigned*>(lds + L_LIST + __builtin_popcount(pmask & ((1u << lane) - 1)) * 4) = W_O + ((kvh * WGH + lane) * 32 + ((wi + lane) & 31)) * (HD * 4);
    batch(0);
  } else {
#pragma unroll
    for (int k = 0; k < NL; ++k) vo[k] = OOB;
  }
  unsigned ord, msrc, dst;
  {
    const int k = tid >> 5, lr = (k & 3) * 4 + (k >> 2), b = blo + (k >> 2), row = (b >> 2) * 16 + (k & 3) * 4 + (b & 3);
    ord = lr * OROW + x * 16;
    msrc = lr * NW * 4;
    dst = W_O + ((kvh * WGH + wi) * 32 + ((row + wi) & 31)) * (HD * 4) + x * 16;
  }
#pragma unroll
  for (int k = 0; k < NL; ++k) asm volatile("" : "+v"(vo[k]));
  const int d0 = 32 * (x >> 3) + (x & 7);
  unsigned oo = ((cb * NH + kvh * 4 + ch) * HD + d0 + 16 * hw) * 2;
  asm volatile("" : "+v"(ord), "+v"(msrc), "+v"(dst), "+v"(oo));
  {
    const int k = tid >> 2, b = blo + (k >> 2), row = (b >> 2) * 16 + (k & 3) * 4 + (b & 3);
    pro[0] = k < 4 * nseq ? W_O + ((kvh * WGH + wi) * 32 + ((row + wi) & 31)) * (HD * 4) + (tid & 3) * 128 : OOB;
    asm volatile("" : "+v"(pro[0]));
  }
  if (nseq <= 4 && u1 - u0 <= NW * NP) {
    round(I1{}, std::false_type{}, 0, true);
    store_o(I1{});
  } else {
    int rd = 0;
    for (;;) {
      asm volatile("" : "+s"(rd));
      const bool last = rd == 1 || u1 - u0 <= NW * NP;
      round(I2{}, std::true_type{}, rd, last);
      if (last) break;
      rd = 1;
      issue_k(1);
    }
    store_o(I2{});
  }
  u32x4 ov[NL];
  if (wave == NW - 1) {
#pragma unroll
    for (int k = 0; k < NL; ++k) ov[k] = __builtin_amdgcn_raw_buffer_load_b128(wrs, vo[k], 0, 16);
  }
  asm volatile("" ::"v"(f2));
  STAMP(8);
  asm volatile("s_waitcnt lgkmcnt(0)\n\ts_barrier" ::: "memory");
  STAMP(9);
  auto publish = [&](auto hc) __attribute__((always_inline)) {
    constexpr int h = decltype(hc)::value;
    f32x4 ow[NW];
    const f32x4 la = *reinterpret_cast<const f32x4*>(lds + L_L + h * 16 * NW * 4 + msrc), lb = *reinterpret_cast<const f32x4*>(lds + L_L + h * 16 * NW * 4 + 16 + msrc);
    const f32x4 ma = *reinterpret_cast<const f32x4*>(lds + L_M + h * 16 * NW * 4 + msrc), mb = *reinterpret_cast<const f32x4*>(lds + L_M + h * 16 * NW * 4 + 16 + msrc);
#pragma unroll
    for (int w = 0; w < NW; ++w) ow[w] = *reinterpret_cast<const f32x4*>(lds + w * WREG + h * 16 * OROW + ord);
    const float Mx = max8f(ma, mb);
    const float L = ((la[0] + la[1]) + (la[2] + la[3])) + ((lb[0] + lb[1]) + (lb[2] + lb[3]));
    const f32x4 o = ((ow[0] + ow[1]) + (ow[2] + ow[3])) + ((ow[4] + ow[5]) + (ow[6] + ow[7]));
    const unsigned t = tag32 | ((__float_as_uint(Mx) >> x) & 1u) | ((__float_as_uint(L) >> x) & 1u) << 8;
    const u32x4 e = {__builtin_amdgcn_perm(__float_as_uint(o[0]), t, 0x07060500u), __builtin_amdgcn_perm(__float_as_uint(o[1]), t, 0x07060501u),
                     __builtin_amdgcn_perm(__float_as_uint(o[2]), t, 0x07060502u), __builtin_amdgcn_perm(__float_as_uint(o[3]), t, 0x07060503u)};
    __builtin_amdgcn_raw_buffer_store_b128(e, wrs, dst ^ (h * 16 * HD * 4), 0, 16);
  };
  if (2 * wave < 4 * nseq) {
    publish(std::integral_constant<int, 0>{});
    if (2 * wave + 16 < 4 * nseq) publish(I1{});
  }
  STAMP(10);
  if (wave == NW - 1) {
    float Mx = -INFINITY, L = 0.f;
    f32x4 o = {0.f, 0.f, 0.f, 0.f};
    int r0 = 0;
    int spins = SPIN;
    for (;;) {
      float mk[NL], lk[NL];
      auto take = [&](auto kc) __attribute__((always_inline)) {
        constexpr int k = decltype(kc)::value;
        const unsigned msk = vo[k] < OOB ? TMASK : 0u;
        unsigned tr = trailer(ov[k]);
        if (__builtin_expect(__ballot(((tr ^ tag32) & msk) != 0) != 0, 0)) {
          do {
            asm volatile("" ::: "memory");
#pragma unroll
            for (int j = k; j >= 0; --j) ov[j] = __builtin_amdgcn_raw_buffer_load_b128(wrs, vo[j], 0, 16);
#pragma unroll
            for (int j = k; j >= 0; --j) asm volatile("" : "+v"(ov[j]));
            tr = trailer(ov[k]);
            --spins;
          } while ((__ballot(((tr ^ tag32) & msk) != 0) != 0) & (spins > 0));
        }
        const u64 bm = __ballot(tr & 1), bl = __ballot(tr & 256);
        mk[k] = msk ? __uint_as_float((unsigned)(bm >> (lane & 32))) : -INFINITY;
        lk[k] = __uint_as_float((unsigned)(bl >> (lane & 32)));
#pragma unroll
        for (int e = 0; e < 4; ++e) ov[k][e] &= 0xFFFFFF00u;
        asm volatile("" : "+v"(ov[k]), "+v"(mk[k]), "+v"(lk[k]));
      };
      take(std::integral_constant<int, 5>{});
      take(std::integral_constant<int, 4>{});
      take(std::integral_constant<int, 3>{});
      take(std::integral_constant<int, 2>{});
      take(std::integral_constant<int, 1>{});
      take(std::integral_constant<int, 0>{});
      STAMP(12);
      float mn = max3f(max3f(max3f(Mx, mk[0], mk[1]), mk[2], mk[3]), mk[4], mk[5]);
      {
        auto s = __builtin_amdgcn_permlane32_swap(__float_as_uint(mn), __float_as_uint(mn), false, false);
        mn = max3f(__uint_as_float(s[0]), __uint_as_float(s[1]), __uint_as_float(s[1]));
      }
      const float ms = (mn == -INFINITY) ? 0.f : mn;
      const float alpha = __builtin_amdgcn_exp2f(Mx - ms);
      Mx = mn;
      L *= alpha;
      o *= alpha;
#pragma unroll
      for (int k = 0; k < NL; ++k) {
        const float w = __builtin_amdgcn_exp2f(mk[k] - ms);
        L = fmaf(w, lk[k], L);
        o += w * __builtin_bit_cast(f32x4, ov[k]);
      }
      r0 += 2 * NL;
      if (__builtin_expect(r0 >= nprod, 1)) break;
      batch(r0);
#pragma unroll
      for (int k = 0; k < NL; ++k) ov[k] = __builtin_amdgcn_raw_buffer_load_b128(wrs, vo[k], 0, 16);
    }
    {
      auto s = __builtin_amdgcn_permlane32_swap(__float_as_uint(L), __float_as_uint(L), false, false);
      L = __uint_as_float(s[0]) + __uint_as_float(s[1]);
    }
#pragma unroll
    for (int r = 0; r < 4; ++r) {
      auto s = __builtin_amdgcn_permlane32_swap(__float_as_uint(o[r]), __float_as_uint(o[r]), false, false);
      o[r] = __uint_as_float(s[0]) + __uint_as_float(s[1]);
    }
    const int lb = clen;
    if (__builtin_expect(lb < LK && !(__builtin_amdgcn_readfirstlane(__float_as_int(Mx * LN2 >= MASKV + 256.f ? 1.f : 0.f))), 0)) {
      const float Mn = Mx * LN2, Mt = fmaxf(Mn, MASKV);
      const float wv = (L > 0.f) ? __expf(Mn - Mt) : 0.f, wm = __expf(MASKV - Mt);
      f32x4 sv = {0.f, 0.f, 0.f, 0.f};
      for (int pos = lb; pos < LK; ++pos) {
        const int phys = *reinterpret_cast<const int*>(lds + L_BT + (cb * NPG + (pos >> 4)) * 4);
        const unsigned short* vp = vc + ((size_t)phys * PS + (pos & 15)) * (NKV * HD) + kvh * HD + d0;
#pragma unroll
        for (int r = 0; r < 4; ++r) sv[r] += __uint_as_float((unsigned)vp[8 * r] << 16);
      }
      o = o * wv + sv * wm;
      L = L * wv + wm * (float)(LK - lb);
    }
    const float inv = __builtin_amdgcn_rcpf(L);
    STAMP(13);
    unsigned short* op = reinterpret_cast<unsigned short*>(reinterpret_cast<unsigned char*>(out) + oo);
    const unsigned ob = pk_bf16((hw ? o[2] : o[0]) * inv, (hw ? o[3] : o[1]) * inv);
    op[0] = (unsigned short)ob;
    op[8] = (unsigned short)(ob >> 16);
  }
  if (wi == 0 && wave == 0) {
    const unsigned flo = lane < 32 ? W_FLAG + (kvh * 32 + lane) * 128 : OOB;
#pragma nounroll
    for (unsigned it = 0; it < SPIN; ++it) {
      const u32x2 fl = __builtin_amdgcn_raw_buffer_load_b64(wrs, flo, 0, 16);
      if (__all((lane >= 32) | ((fl[0] == (unsigned)tag) & (fl[1] == (unsigned)(tag >> 32))))) break;
      asm volatile("s_sleep 2" ::: "memory");
    }
    __builtin_amdgcn_raw_buffer_store_b128(tag4, wrs, lane < 8 ? lane * 16 : OOB, W_EPOCH + kvh * 128, 16);
  }
  STAMP_FLUSH(ws, blockIdx.x * NW + wave);
}

}

extern "C" void kernel_launch(void* const* d_in, const int* in_sizes, int n_in, void* d_out, int out_size, void* d_ws, size_t ws_size,
                              hipStream_t stream) {
  attn_kernel<<<NKV * WGH, NW * 64, 0, stream>>>((const unsigned short*)d_in[0], (const unsigned short*)d_in[1], (const unsigned short*)d_in[2],
                                                 (const int*)d_in[3], (const int*)d_in[4], (unsigned short*)d_out, (unsigned char*)d_ws,
                                                 (unsigned)in_sizes[1] * 2u);
}
```
